```python
import torch

def _softmax(x, axis=-1):
    return torch.softmax(x, dim=axis)
def _gelu(x):
    return torch.nn.functional.gelu(x, approximate="tanh")
def _silu(x):
    return torch.nn.functional.silu(x)
def _sigmoid(x):
    return torch.sigmoid(x)
def _layernorm(x, axis=-1, eps=1e-5):
    m = x.mean(dim=axis, keepdim=True)
    v = x.var(dim=axis, keepdim=True, unbiased=False)
    return (x - m) / torch.sqrt(v + eps)
def _scatter_add(tgt, idx, src, axis=0):
    return tgt.clone().index_add_(axis, idx, src)
def _segment_sum(src, seg, n):
    out = torch.zeros((n,) + src.shape[1:], dtype=src.dtype)
    return out.index_add_(0, seg, src)
def _pad(x, pw):
    flat = tuple(v for pair in reversed(pw) for v in pair)
    return torch.nn.functional.pad(x, flat)

kernel_name = 'recurrent_scan__linear_kernel_hedgehog__dense_activation_silu__prompt'

def setup_inputs():

    _w = (lambda a: (lambda t: t.to(torch.bfloat16) if t.is_floating_point() else t)(torch.as_tensor(a)))
    def _A_setup():
        B, S, D, H = 1, 2048, 640, 32
        Dh = D // H
        return {
            "x": _w((lambda *s: torch.randn(*s, dtype=torch.bfloat16) * 0.05)(B, S, D)),
            "wqkv": _w((lambda *s: torch.randn(*s, dtype=torch.bfloat16) * 0.4)(D, 3 * D)),
            "w_phi": _w((lambda *s: torch.randn(*s, dtype=torch.bfloat16) * 0.4)(H, Dh, Dh) * (Dh ** -0.5)),
            "wo": _w((lambda *s: torch.randn(*s, dtype=torch.bfloat16) * 0.2)(D, D)),
        }
    _A_in = _A_setup()

    def _B_setup():
        B, S, D, F = 1, 2048, 640, 1728
        return {
            "x": _w((lambda *s: torch.randn(*s, dtype=torch.bfloat16) * 0.05)(B, S, D)),
            "w1": _w((lambda *s: torch.randn(*s, dtype=torch.bfloat16) * 0.4)(D, F) * (D ** -0.5)),
            "w2": _w((lambda *s: torch.randn(*s, dtype=torch.bfloat16) * 0.1)(F, D) * (F ** -0.5)),
        }
    _B_in = _B_setup()

    return {
        "A_x": _A_in["x"],
        "A_wqkv": _A_in["wqkv"],
        "A_w_phi": _A_in["w_phi"],
        "A_wo": _A_in["wo"],
        "B_x": _B_in["x"],
        "B_w1": _B_in["w1"],
        "B_w2": _B_in["w2"],
    }

def reference(A_x, A_wqkv, A_w_phi, A_wo, B_x, B_w1, B_w2):
    def _A_ref(x, wqkv, w_phi, wo):
        B, S, D = x.shape
        H = 32; Dh = D // H
        qkv = (lambda x, *d: x.permute(*d))((x @ wqkv).reshape(B, S, 3, H, Dh), 2, 0, 3, 1, 4)
        q, k, v = qkv[0], qkv[1], qkv[2]
        pq = torch.einsum("bhsd,hdf->bhsf", q, w_phi)
        pk = torch.einsum("bhsd,hdf->bhsf", k, w_phi)
        phi_q = torch.concatenate([_softmax(pq, axis=-1), _softmax(-pq, axis=-1)], axis=-1)
        phi_k = torch.concatenate([_softmax(pk, axis=-1), _softmax(-pk, axis=-1)], axis=-1)
        kv = torch.einsum("bhsf,bhsd->bhfd", phi_k, v)
        z  = torch.einsum("bhsf->bhf", phi_k)
        num = torch.einsum("bhqf,bhfd->bhqd", phi_q, kv)
        den = torch.einsum("bhqf,bhf->bhq", phi_q, z)[..., None] + 1e-6
        o = num / den
        return (lambda x, *d: x.permute(*d))(o, 0, 2, 1, 3).reshape(B, S, D) @ wo

    def _B_ref(x, w1, w2):
        h = torch.einsum("bsd,df->bsf", x, w1)
        h = _silu(h)
        out = torch.einsum("bsf,fd->bsd", h, w2)
        return out

    x = A_x
    xn = _layernorm(x, axis=-1)
    s = _A_ref(xn, A_wqkv, A_w_phi, A_wo)
    g = _silu(_B_ref(xn, B_w1, B_w2))
    return x + s * g
```

```cpp
#include <hip/hip_runtime.h>
#include <hip/hip_bf16.h>
typedef __hip_bfloat16 bf16;
constexpr int S=2048, D=640, H=32, DH=20, F=1728;
__device__ __forceinline__ float b2f(bf16 v){ return __bfloat162float(v); }
__global__ void k_ln(const bf16* x, float* xn){
  int r = blockIdx.x; float s=0, q=0;
  for (int i=threadIdx.x;i<D;i+=64){ float v=b2f(x[r*D+i]); s+=v; }
  for (int o=32;o;o>>=1) s += __shfl_xor(s,o);
  float m = s/D;
  for (int i=threadIdx.x;i<D;i+=64){ float v=b2f(x[r*D+i])-m; q+=v*v; }
  for (int o=32;o;o>>=1) q += __shfl_xor(q,o);
  float rs = rsqrtf(q/D+1e-5f);
  for (int i=threadIdx.x;i<D;i+=64) xn[r*D+i] = (b2f(x[r*D+i])-m)*rs;
}
__global__ void k_gemm(const float* A, const bf16* B, float* C, int N, int K, int act){
  __shared__ float As[16][17], Bs[16][17];
  int tx=threadIdx.x, ty=threadIdx.y; int r=blockIdx.y*16+ty, c=blockIdx.x*16+tx; float acc=0;
  for (int k0=0;k0<K;k0+=16){ As[ty][tx]=A[r*K+k0+tx]; Bs[ty][tx]=b2f(B[(k0+ty)*N+c]); __syncthreads();
    for(int k=0;k<16;++k) acc += As[ty][k]*Bs[k][tx]; __syncthreads(); }
  if (act) acc = acc/(1.f+__expf(-acc));
  C[r*N+c]=acc;
}
__global__ void k_phi(const float* qkv, const bf16* wphi, float* phi){
  int i = blockIdx.x*blockDim.x+threadIdx.x; if (i>=2*S*H) return;
  int h=i%H, s=(i/H)%S, w=i/(H*S);
  float p[DH]; float mx=-1e30f, mn=1e30f;
  for(int f=0;f<DH;++f){ float a=0; for(int d=0;d<DH;++d) a += qkv[s*3*D + w*D + h*DH + d]*b2f(wphi[h*DH*DH+d*DH+f]); p[f]=a; mx=fmaxf(mx,a); mn=fminf(mn,a);}
  float s1=0,s2=0; for(int f=0;f<DH;++f){ s1+=__expf(p[f]-mx); s2+=__expf(mn-p[f]); }
  float* o = phi + ((size_t)(w*S+s)*H+h)*40;
  for(int f=0;f<DH;++f){ o[f]=__expf(p[f]-mx)/s1; o[20+f]=__expf(mn-p[f])/s2; }
}
__global__ void k_kv(const float* phi, const float* qkv, float* kv){
  int i = blockIdx.x*blockDim.x+threadIdx.x; if (i>=H*40*21) return;
  int d=i%21, f=(i/21)%40, h=i/(840); float a=0;
  for(int s=0;s<S;++s){ float p=phi[((size_t)(S+s)*H+h)*40+f]; a += p*(d<20? qkv[s*3*D+2*D+h*DH+d]:1.f);} kv[i]=a;
}
__global__ void k_o(const float* phi, const float* kv, float* o){
  int i = blockIdx.x*blockDim.x+threadIdx.x; if (i>=S*D) return;
  int d=i%DH, h=(i/DH)%H, s=i/D; float n=0, dn=0;
  for(int f=0;f<40;++f){ float p=phi[((size_t)s*H+h)*40+f]; n+=p*kv[h*840+f*21+d]; dn+=p*kv[h*840+f*21+20]; }
  o[i]=n/(dn+1e-6f);
}
__global__ void k_fin(const bf16* x, const float* s, const float* g, bf16* out){
  int i = blockIdx.x*blockDim.x+threadIdx.x; if (i>=S*D) return;
  out[i]=__float2bfloat16(b2f(x[i])+s[i]*g[i]);
}
extern "C" void kernel_launch(void* const* d_in, const int* in_sizes, int n_in, void* d_out, int out_size, void* d_ws, size_t ws_size, hipStream_t stream){
  const bf16* x=(const bf16*)d_in[0]; const bf16* wqkv=(const bf16*)d_in[1]; const bf16* wphi=(const bf16*)d_in[2]; const bf16* wo=(const bf16*)d_in[3];
  const bf16* w1=(const bf16*)d_in[5]; const bf16* w2=(const bf16*)d_in[6];
  float* ws=(float*)d_ws; float* xn=ws; float* qkv=xn+S*D; float* h=qkv+S*3*D; float* phi=h+S*F; float* kv=phi+2*S*H*40; float* o=kv+H*840+64; float* s=o+S*D; float* g=s+S*D;
  k_ln<<<S,64,0,stream>>>(x,xn);
  k_gemm<<<dim3(3*D/16,S/16),dim3(16,16),0,stream>>>(xn,wqkv,qkv,3*D,D,0);
  k_gemm<<<dim3(F/16,S/16),dim3(16,16),0,stream>>>(xn,w1,h,F,D,1);
  k_phi<<<(2*S*H+255)/256,256,0,stream>>>(qkv,wphi,phi);
  k_kv<<<(H*840+63)/64,64,0,stream>>>(phi,qkv,kv);
  k_o<<<(S*D+255)/256,256,0,stream>>>(phi,kv,o);
  k_gemm<<<dim3(D/16,S/16),dim3(16,16),0,stream>>>(o,wo,s,D,D,0);
  k_gemm<<<dim3(D/16,S/16),dim3(16,16),0,stream>>>(h,w2,g,D,F,1);
  k_fin<<<(S*D+255)/256,256,0,stream>>>(x,s,g,(bf16*)d_out);
}
```

```cpp
#include <hip/hip_runtime.h>
#include <stdint.h>

typedef __attribute__((ext_vector_type(8))) __bf16 bf16x8;
typedef __attribute__((ext_vector_type(2))) __bf16 bf16x2;
typedef __attribute__((ext_vector_type(4))) short s16x4;
typedef __attribute__((ext_vector_type(4))) float f32x4;
typedef __attribute__((ext_vector_type(2))) float f32x2;
typedef __attribute__((ext_vector_type(4))) unsigned u32x4;
typedef __attribute__((ext_vector_type(2))) unsigned u32x2;
#define LDSP(T) __attribute__((address_space(3))) T*
#define GLBP(T) __attribute__((address_space(1))) T*

constexpr size_t WS_HF  = 0;
constexpr size_t WS_OF  = WS_HF + 128ull * 54 * 1024;
constexpr size_t WS_BF  = WS_OF + 128ull * 20 * 1024;
constexpr size_t WS_PQ  = WS_BF + 8ull * 74 * 5 * 1024;
constexpr size_t WS_PQX = WS_PQ + 32ull * 128 * 1024;
constexpr size_t WS_KVP = WS_PQX + 32ull * 128 * 256;
constexpr size_t WS_CNT = WS_KVP + 32ull * 16 * 3360;
constexpr int STG = 49152;
constexpr int L_WPHI = 3 * STG;
constexpr int L_CS = L_WPHI + 7168;
constexpr int L_ST = L_CS + 1024;
constexpr int K1_LDS = L_ST + 1024;
constexpr int RS = 152;
constexpr int KVI = 128 * RS;

__device__ __forceinline__ unsigned pk2(float a, float b) {
  f32x2 v = {a, b};
  bf16x2 r = __builtin_convertvector(v, bf16x2);
  return __builtin_bit_cast(unsigned, r);
}
__device__ __forceinline__ f32x4 mfma32(u32x4 a, u32x4 b, f32x4 c) {
  return __builtin_amdgcn_mfma_f32_16x16x32_bf16(__builtin_bit_cast(bf16x8, a), __builtin_bit_cast(bf16x8, b), c, 0, 0, 0);
}
__device__ __forceinline__ f32x4 mfma16(u32x2 a, u32x2 b, f32x4 c) {
  return __builtin_amdgcn_mfma_f32_16x16x16bf16_1k(__builtin_bit_cast(s16x4, a), __builtin_bit_cast(s16x4, b), c, 0, 0, 0);
}
__device__ __forceinline__ u32x2 trd(unsigned addr) {
  s16x4 v = __builtin_amdgcn_ds_read_tr16_b64_v4i16((LDSP(s16x4))addr);
  return __builtin_bit_cast(u32x2, v);
}
__device__ __forceinline__ u32x4 trd2(unsigned a0, unsigned a1) {
  u32x2 lo = trd(a0), hi = trd(a1);
  u32x4 r = {lo.x, lo.y, hi.x, hi.y};
  return r;
}
__device__ __forceinline__ u32x4 lds128(unsigned addr) { return *(LDSP(u32x4))addr; }
__device__ __forceinline__ void glds16(const char* g, unsigned ldsbase) {
  __builtin_amdgcn_global_load_lds((GLBP(const void))g, (LDSP(void))ldsbase, 16, 0, 0);
}
__device__ __forceinline__ float gmax(float a) {
  unsigned u = __builtin_bit_cast(unsigned, a);
  auto r = __builtin_amdgcn_permlane16_swap(u, u, false, false);
  float m = fmaxf(__builtin_bit_cast(float, (unsigned)r[0]), __builtin_bit_cast(float, (unsigned)r[1]));
  u = __builtin_bit_cast(unsigned, m);
  auto r2 = __builtin_amdgcn_permlane32_swap(u, u, false, false);
  return fmaxf(__builtin_bit_cast(float, (unsigned)r2[0]), __builtin_bit_cast(float, (unsigned)r2[1]));
}
__device__ __forceinline__ float gsum(float a) {
  unsigned u = __builtin_bit_cast(unsigned, a);
  auto r = __builtin_amdgcn_permlane16_swap(u, u, false, false);
  float m = __builtin_bit_cast(float, (unsigned)r[0]) + __builtin_bit_cast(float, (unsigned)r[1]);
  u = __builtin_bit_cast(unsigned, m);
  auto r2 = __builtin_amdgcn_permlane32_swap(u, u, false, false);
  return __builtin_bit_cast(float, (unsigned)r2[0]) + __builtin_bit_cast(float, (unsigned)r2[1]);
}
__device__ __forceinline__ float silu_f(float v) {
  float e = __builtin_amdgcn_exp2f(-1.4426950408889634f * v);
  return v * __builtin_amdgcn_rcpf(1.0f + e);
}
#define WAITVM(n) asm volatile("s_waitcnt vmcnt(" #n ")" ::: "memory")
#define WAITLGKM0() asm volatile("s_waitcnt lgkmcnt(0)" ::: "memory")
#define BAR() __builtin_amdgcn_s_barrier()

__device__ __forceinline__ void hedge(f32x4 p, f32x4 px, float sc, int g, u32x4& mainv, unsigned& extv) {
  float mx = fmaxf(fmaxf(fmaxf(p[0], p[1]), fmaxf(p[2], p[3])), fmaxf(fmaxf(px[0], px[1]), fmaxf(px[2], px[3])));
  float mn = fminf(fminf(fminf(p[0], p[1]), fminf(p[2], p[3])), fminf(fminf(px[0], px[1]), fminf(px[2], px[3])));
  mx = gmax(mx);
  mn = -gmax(-mn);
  float xe = g == 0 ? px[0] : (g == 1 ? px[1] : (g == 2 ? px[2] : px[3]));
  float bp = -mx * sc, bn = mn * sc;
  float ep[5], en[5];
#pragma unroll
  for (int r = 0; r < 4; ++r) {
    ep[r] = __builtin_amdgcn_exp2f(__builtin_fmaf(p[r], sc, bp));
    en[r] = __builtin_amdgcn_exp2f(__builtin_fmaf(p[r], -sc, bn));
  }
  ep[4] = __builtin_amdgcn_exp2f(__builtin_fmaf(xe, sc, bp));
  en[4] = __builtin_amdgcn_exp2f(__builtin_fmaf(xe, -sc, bn));
  float sp = (ep[0] + ep[1]) + (ep[2] + ep[3]) + ep[4];
  float sn = (en[0] + en[1]) + (en[2] + en[3]) + en[4];
  sp = __builtin_amdgcn_rcpf(gsum(sp));
  sn = __builtin_amdgcn_rcpf(gsum(sn));
  mainv[0] = pk2(ep[0] * sp, ep[1] * sp);
  mainv[1] = pk2(ep[2] * sp, ep[3] * sp);
  mainv[2] = pk2(en[0] * sn, en[1] * sn);
  mainv[3] = pk2(en[2] * sn, en[3] * sn);
  extv = pk2(ep[4] * sp, en[4] * sn);
}

template <int TYPE>
__device__ __forceinline__ void k1_body(const char* __restrict__ x, const char* __restrict__ wqkv, const char* __restrict__ wphi,
                                        const char* __restrict__ wo, const char* __restrict__ w1, const char* __restrict__ w2,
                                        char* __restrict__ ws, int rb, int jj, int bid) {
  constexpr int NT = TYPE == 2 ? 8 : 7;
  const int tid = threadIdx.x, l = tid & 63;
  const int w = __builtin_amdgcn_readfirstlane(tid >> 6);
  const int tg = w & 3, nh = w >> 2;
  const int g = l >> 4, lc = l & 15, q = (l >> 2) & 3, p = l & 3;
  const char* xp;
  {
    int row = 8 * w + (l >> 3);
    xp = x + (size_t)(rb * 128 + row) * 1280 + (((l & 7) ^ (l >> 3)) << 4);
  }
  const char* wp; int ld;
  {
    int row = 2 * w + (l >> 5);
    int sl = (l & 31) ^ (((row & 3) << 1) | (((row >> 3) & 1) << 3));
    if (TYPE == 0) {
      if (sl >= 24) { wp = w1 + jj * 128 + (sl - 24) * 16; ld = 3456; }
      else { int s2 = sl < 11 ? (sl > 9 ? 9 : sl) : (sl > 20 ? 9 : sl - 11);
             wp = wqkv + (sl < 11 ? 1280 : 2560) + jj * 160 + s2 * 16; ld = 3840; }
    } else if (TYPE == 1) {
      if (sl >= 24) { wp = w1 + (8 + jj) * 128 + (sl - 24) * 16; ld = 3456; }
      else { int s2 = sl > 19 ? 19 : sl; wp = wqkv + jj * 320 + s2 * 16; ld = 3840; }
    } else {
      int L = 12 + 4 * jj + (sl >> 3); L = L > 26 ? 26 : L;
      wp = w1 + L * 128 + (sl & 7) * 16; ld = 3456;
    }
    wp += (size_t)row * ld;
  }
  auto issue = [&](int s, int buf) {
    unsigned b = buf * STG;
    const char* xs = xp + s * 128;
    glds16(xs, b + w * 1024);
    glds16(xs + 64 * 1280, b + 8192 + w * 1024);
    const char* ws_ = wp + (size_t)(s * 64) * ld;
#pragma unroll
    for (int m = 0; m < 4; ++m) glds16(ws_ + (size_t)(16 * m) * ld, b + 16384 + (w + 8 * m) * 1024);
  };
  if (TYPE != 2) {
    constexpr int NCH = TYPE == 0 ? 200 : 400;
    if (w * 64 < NCH) {
      int c = tid < NCH ? tid : NCH - 1;
      glds16(wphi + (size_t)jj * (NCH * 16) + c * 16, L_WPHI + w * 1024);
    }
  }
  issue(0, 0); issue(1, 1); issue(2, 2);
  unsigned ta[NT], ca[2];
  {
    unsigned rowb = 16384 + (8 * g + q) * 512, sw = (q << 2) | ((g & 1) << 4);
    auto grp = [&](int t) -> int {
      if (TYPE == 0) {
        if (t < 2) return 5 * (2 * nh + t) + p;
        if (t == 2) return (p >> 1) * 22 + 10 * nh + 5 * (p & 1) + 4;
        if (t < 5) return 22 + 5 * (2 * nh + t - 3) + p;
        return 48 + 4 * (2 * nh + t - 5) + p;
      } else if (TYPE == 1) {
        if (t < 4) return 5 * (4 * nh + t) + p;
        if (t == 4) return 20 * nh + 5 * p + 4;
        return 48 + 4 * (2 * nh + t - 5) + p;
      } else return 32 * nh + 4 * t + p;
    };
#pragma unroll
    for (int t = 0; t < NT; ++t) ta[t] = rowb + ((grp(t) ^ sw) << 3);
#pragma unroll
    for (int c = 0; c < 2; ++c) {
      int t = 2 * tg + c; t = t >= NT ? NT - 1 : t;
      ca[c] = rowb + ((grp(t) ^ sw) << 3);
    }
  }
  unsigned xa[2][2];
#pragma unroll
  for (int tt = 0; tt < 2; ++tt)
#pragma unroll
    for (int k2 = 0; k2 < 2; ++k2) {
      int row = 32 * tg + 16 * (tt ^ nh) + lc;
      xa[tt][k2] = row * 128 + (((4 * k2 + g) ^ (row & 7)) << 4);
    }
  f32x4 acc[NT][2], cs[2], rsum, rsq;
#pragma unroll
  for (int t = 0; t < NT; ++t) { acc[t][0] = (f32x4)(0.f); acc[t][1] = (f32x4)(0.f); }
  cs[0] = cs[1] = rsum = rsq = (f32x4)(0.f);
  const u32x4 ones = {0x3f803f80u, 0x3f803f80u, 0x3f803f80u, 0x3f803f80u};
  const int nside = (bid + 256 < 370) ? 2 : 1;
  auto side_issue = [&](int u, unsigned lb) {
    int ks = u / 5, cb = u - ks * 5;
    int v = 4 * w + (l >> 4);
    const char* src;
    if (ks < 54) src = w2 + (size_t)(32 * ks + v) * 1280;
    else {
      int k2 = ks - 54, hg = k2 / 5, kk = k2 - hg * 5, row;
      if (kk < 4) row = 20 * (8 * hg + 2 * kk + (v >> 4)) + (v & 15);
      else row = 20 * (8 * hg + (v & 3) + 4 * (v >> 4)) + 16 + ((v & 15) >> 2);
      src = wo + (size_t)row * 1280;
    }
    glds16(src + cb * 256 + (((l & 15) ^ ((v & 7) << 1)) << 4), lb + w * 1024);
  };
#pragma unroll
  for (int s = 0; s < 10; ++s) {
    if (s == 0) WAITVM(12); else if (s < 9) WAITVM(6); else WAITVM(2);
    BAR();
    if (s >= 1 && s <= 7) issue(s + 2, (s + 2) % 3);
    if (s == 8) { side_issue(bid, STG); side_issue(nside == 2 ? bid + 256 : bid, STG + 8192); }
    const unsigned b = (s % 3) * STG;
#pragma unroll
    for (int k2 = 0; k2 < 2; ++k2) {
      u32x4 xf0 = lds128(b + xa[0][k2]), xf1 = lds128(b + xa[1][k2]);
#pragma unroll
      for (int t = 0; t < NT; ++t) {
        u32x4 wf = trd2(b + ta[t] + k2 * 16384, b + ta[t] + k2 * 16384 + 2048);
        acc[t][0] = mfma32(wf, xf0, acc[t][0]);
        acc[t][1] = mfma32(wf, xf1, acc[t][1]);
      }
#pragma unroll
      for (int c = 0; c < 2; ++c) {
        u32x4 wf = trd2(b + ca[c] + k2 * 16384, b + ca[c] + k2 * 16384 + 2048);
        cs[c] = mfma32(wf, ones, cs[c]);
      }
      rsum = mfma32(ones, xf0, rsum);
      rsq = mfma32(xf0, xf0, rsq);
    }
    WAITLGKM0();
  }
  if (lc == 0) {
    *(LDSP(f32x4))(L_CS + nh * 512 + (2 * tg) * 64 + g * 16) = cs[0];
    *(LDSP(f32x4))(L_CS + nh * 512 + (2 * tg + 1) * 64 + g * 16) = cs[1];
  }
  if (g == (lc >> 2)) {
    float sq = p == 0 ? rsq[0] : (p == 1 ? rsq[1] : (p == 2 ? rsq[2] : rsq[3]));
    float mean = rsum[0] * (1.0f / 640.0f);
    float var = fmaxf(sq * (1.0f / 640.0f) - mean * mean, 0.f);
    f32x2 st = {mean, __builtin_amdgcn_rsqf(var + 1e-5f)};
    *(LDSP(f32x2))(L_ST + (32 * tg + 16 * nh + lc) * 8) = st;
  }
  WAITVM(0);
  WAITLGKM0();
  BAR();
  float mean[2], rstd[2];
#pragma unroll
  for (int tt = 0; tt < 2; ++tt) {
    f32x2 st = *(LDSP(f32x2))(L_ST + (32 * tg + 16 * (tt ^ nh) + lc) * 8);
    mean[tt] = st[0]; rstd[tt] = st[1];
  }
#pragma unroll
  for (int t = 0; t < NT; ++t) {
    f32x4 c4 = *(LDSP(f32x4))(L_CS + nh * 512 + t * 64 + g * 16);
#pragma unroll
    for (int tt = 0; tt < 2; ++tt)
#pragma unroll
      for (int r = 0; r < 4; ++r) acc[t][tt][r] = __builtin_fmaf(-mean[tt], c4[r], acc[t][tt][r]);
  }
  {
    unsigned a = STG + (4 * g + q) * 256 + (((2 * w + (p >> 1)) ^ (((4 * g + q) & 7) << 1)) << 4) + (p & 1) * 8;
#pragma unroll
    for (int i = 0; i < 2; ++i) {
      if (i < nside) {
        int u = bid + 256 * i, ks = u / 5, cb = u - ks * 5, ct = cb * 8 + w, nb = ct / 5, ci = ct - nb * 5;
        u32x4 f = trd2(a + i * 8192, a + i * 8192 + 4096);
        *(u32x4*)(ws + WS_BF + ((size_t)(nb * 74 + ks) * 5 + ci) * 1024 + l * 16) = f;
      }
    }
  }
  {
    constexpr int T0 = TYPE == 2 ? 0 : 5;
#pragma unroll
    for (int t = T0; t < NT; t += 2) {
      int ks;
      if (TYPE == 0) ks = 2 * jj + nh;
      else if (TYPE == 1) ks = 2 * (8 + jj) + nh;
      else ks = 2 * (12 + 4 * jj) + 4 * nh + (t >> 1);
      if (ks < 54) {
#pragma unroll
        for (int tt = 0; tt < 2; ++tt) {
          float hv[8];
#pragma unroll
          for (int r = 0; r < 4; ++r) { hv[r] = silu_f(acc[t][tt][r] * rstd[tt]); hv[4 + r] = silu_f(acc[t + 1][tt][r] * rstd[tt]); }
          u32x4 o = {pk2(hv[0], hv[1]), pk2(hv[2], hv[3]), pk2(hv[4], hv[5]), pk2(hv[6], hv[7])};
          int gtt = rb * 8 + 2 * tg + (tt ^ nh);
          *(u32x4*)(ws + WS_HF + ((size_t)gtt * 54 + ks) * 1024 + l * 16) = o;
        }
      }
    }
  }
  if (TYPE == 2) return;
  constexpr int NHD = TYPE == 0 ? 2 : 4;
  constexpr int XT = NHD;
  u32x2 xb[2];
#pragma unroll
  for (int tt = 0; tt < 2; ++tt) { xb[tt][0] = pk2(acc[XT][tt][0], acc[XT][tt][1]); xb[tt][1] = pk2(acc[XT][tt][2], acc[XT][tt][3]); }
#pragma unroll
  for (int hh = 0; hh < NHD; ++hh) {
    const int hl = NHD * nh + hh;
    unsigned wb = L_WPHI + hl * 800;
    u32x4 a0 = trd2(wb + (4 * g + q) * 40 + p * 8, wb + (16 + q) * 40 + p * 8);
    u32x4 a1 = trd2(wb + (4 * g + q) * 40 + 32, wb + (16 + q) * 40 + 32);
    if (g != hh) { a0[2] = 0; a0[3] = 0; a1[2] = 0; a1[3] = 0; }
#pragma unroll
    for (int tt = 0; tt < 2; ++tt) {
      u32x4 kb = {pk2(acc[hh][tt][0], acc[hh][tt][1]), pk2(acc[hh][tt][2], acc[hh][tt][3]), xb[tt][0], xb[tt][1]};
      f32x4 pm = mfma32(a0, kb, (f32x4)(0.f));
      f32x4 px = mfma32(a1, kb, (f32x4)(0.f));
      u32x4 mv; unsigned ev;
      hedge(pm, px, rstd[tt] * 1.4426950408889634f, g, mv, ev);
      if (TYPE == 1) {
        int gtt = rb * 8 + 2 * tg + (tt ^ nh);
        int head = jj * 8 + hl;
        *(u32x4*)(ws + WS_PQ + ((size_t)head * 128 + gtt) * 1024 + l * 16) = mv;
        *(unsigned*)(ws + WS_PQX + ((size_t)head * 128 + gtt) * 256 + l * 4) = ev;
      } else {
        unsigned ib = (hl < 2 ? 2 * STG + hl * KVI : (hl - 2) * KVI) + (32 * tg + 16 * (tt ^ nh) + lc) * RS + g * 8;
        u32x2 t0 = {mv[0], mv[1]}, t1 = {mv[2], mv[3]}, t2 = {ev, 0u};
        *(LDSP(u32x2))(ib) = t0;
        *(LDSP(u32x2))(ib + 32) = t1;
        *(LDSP(u32x2))(ib + 64) = t2;
        u32x2 vv = {pk2(acc[3 + hh][tt][0] * rstd[tt], acc[3 + hh][tt][1] * rstd[tt]), pk2(acc[3 + hh][tt][2] * rstd[tt], acc[3 + hh][tt][3] * rstd[tt])};
        *(LDSP(u32x2))(ib + 96) = vv;
      }
    }
  }
  if (TYPE == 0) {
#pragma unroll
    for (int tt = 0; tt < 2; ++tt) {
      int hl = 2 * nh + (g & 1);
      unsigned ib = (hl < 2 ? 2 * STG + hl * KVI : (hl - 2) * KVI) + (32 * tg + 16 * (tt ^ nh) + lc) * RS + (g >= 2 ? 128 : 136);
      u32x2 vv = {pk2(acc[2][tt][0] * rstd[tt], acc[2][tt][1] * rstd[tt]), pk2(acc[2][tt][2] * rstd[tt], acc[2][tt][3] * rstd[tt])};
      if (g < 2) { vv[0] = 0x00003f80u; vv[1] = 0u; }
      *(LDSP(u32x2))(ib) = vv;
    }
    WAITLGKM0();
    BAR();
    const int hl = w >> 1, dt = w & 1;
    unsigned ib = (hl < 2 ? 2 * STG + hl * KVI : (hl - 2) * KVI) + (4 * g + q) * RS + p * 8;
    f32x4 kv0 = (f32x4)(0.f), kv1 = (f32x4)(0.f), kv2 = (f32x4)(0.f);
#pragma unroll
    for (int k = 0; k < 4; ++k) {
      unsigned a = ib + k * 32 * RS;
      u32x4 bv = trd2(a + 96 + dt * 32, a + 96 + dt * 32 + 16 * RS);
      kv0 = mfma32(trd2(a, a + 16 * RS), bv, kv0);
      kv1 = mfma32(trd2(a + 32, a + 32 + 16 * RS), bv, kv1);
      kv2 = mfma32(trd2(a + 64, a + 64 + 16 * RS), bv, kv2);
    }
    if (dt == 0 || lc < 5) {
      int col = 16 * dt + lc;
      char* o = ws + WS_KVP + ((size_t)(jj * 4 + hl) * 16 + rb) * 3360;
      *(f32x4*)(o + col * 64 + g * 16) = kv0;
      *(f32x4*)(o + 1344 + col * 64 + g * 16) = kv1;
      f32x2 e = {kv2[0], kv2[1]};
      *(f32x2*)(o + 2688 + col * 32 + g * 8) = e;
    }
  }
}

__global__ __launch_bounds__(512, 2) void k1(const char* __restrict__ x, const char* __restrict__ wqkv, const char* __restrict__ wphi,
                                             const char* __restrict__ wo, const char* __restrict__ w1, const char* __restrict__ w2,
                                             char* __restrict__ ws) {
  const int bid = blockIdx.x, xcd = bid & 7, idx = bid >> 3;
  const int rb = (xcd >> 2) * 8 + (idx >> 2), j = xcd & 3, ty = idx & 3;
  if (bid == 0 && threadIdx.x < 8) *(unsigned*)(ws + WS_CNT + threadIdx.x * 128) = 0u;
  if (ty < 2) k1_body<0>(x, wqkv, wphi, wo, w1, w2, ws, rb, 2 * j + ty, bid);
  else if (ty == 2) k1_body<1>(x, wqkv, wphi, wo, w1, w2, ws, rb, j, bid);
  else k1_body<2>(x, wqkv, wphi, wo, w1, w2, ws, rb, j, bid);
}

constexpr int K2_LDS = 81920;
constexpr int PF = 4;
__global__ __launch_bounds__(256, 1) void k2(const char* __restrict__ x, char* __restrict__ ws, char* __restrict__ out) {
  const int bid = blockIdx.x, tgp = bid & 7, idx = bid >> 3;
  const int mb = 4 * tgp + (idx & 3), nb = idx >> 2;
  const int tid = threadIdx.x, l = tid & 63;
  const int w = __builtin_amdgcn_readfirstlane(tid >> 6);
  const int g = l >> 4, lc = l & 15;
  {
    const int head = idx;
    f32x4 part[16];
    const char* kp = ws + WS_KVP + (size_t)head * 16 * 3360 + (tid < 210 ? tid : 209) * 16;
#pragma unroll
    for (int i = 0; i < 16; ++i) part[i] = *(const f32x4*)(kp + i * 3360);
    u32x4 pq[4]; unsigned px[4];
#pragma unroll
    for (int i = 0; i < 4; ++i) {
      int gtt = tgp * 16 + w * 4 + i;
      pq[i] = *(const u32x4*)(ws + WS_PQ + ((size_t)head * 128 + gtt) * 1024 + l * 16);
      px[i] = *(const unsigned*)(ws + WS_PQX + ((size_t)head * 128 + gtt) * 256 + l * 4);
    }
    f32x4 s = ((part[0] + part[1]) + (part[2] + part[3])) + ((part[4] + part[5]) + (part[6] + part[7])) +
              (((part[8] + part[9]) + (part[10] + part[11])) + ((part[12] + part[13]) + (part[14] + part[15])));
    u32x2 sb = {pk2(s[0], s[1]), pk2(s[2], s[3])};
    if (tid < 210) *(LDSP(u32x2))(tid * 8) = sb;
    __syncthreads();
    int c1 = (lc & 3) == 0 ? 16 + (lc >> 2) : 20;
    u32x2 t0 = *(LDSP(u32x2))(lc * 32 + g * 8), t1 = *(LDSP(u32x2))(672 + lc * 32 + g * 8);
    u32x4 a0 = {t0[0], t0[1], t1[0], t1[1]};
    u32x2 e0 = {*(LDSP(unsigned))(1344 + lc * 16 + g * 4), 0u};
    t0 = *(LDSP(u32x2))(c1 * 32 + g * 8); t1 = *(LDSP(u32x2))(672 + c1 * 32 + g * 8);
    u32x4 a1 = {t0[0], t0[1], t1[0], t1[1]};
    u32x2 e1 = {*(LDSP(unsigned))(1344 + c1 * 16 + g * 4), 0u};
    const int hg = head >> 3, hj = head & 7;
#pragma unroll
    for (int i = 0; i < 4; ++i) {
      u32x2 pe = {px[i], 0u};
      f32x4 d0 = mfma32(a0, pq[i], (f32x4)(0.f));
      f32x4 d1 = mfma32(a1, pq[i], (f32x4)(0.f));
      d0 = mfma16(e0, pe, d0);
      d1 = mfma16(e1, pe, d1);
      float inv = __builtin_amdgcn_rcpf(d1[1] + 1e-6f);
      unsigned long long ov = (unsigned long long)pk2(d0[0] * inv, d0[1] * inv) | ((unsigned long long)pk2(d0[2] * inv, d0[3] * inv) << 32);
      unsigned short oe = (unsigned short)(pk2(d1[0] * inv, 0.f) & 0xffffu);
      int gtt = tgp * 16 + w * 4 + i;
      char* ob = ws + WS_OF + ((size_t)gtt * 20 + hg * 5) * 1024 + l * 16;
      __hip_atomic_store((unsigned long long*)(ob + (hj >> 1) * 1024 + (hj & 1) * 8), ov, __ATOMIC_RELAXED, __HIP_MEMORY_SCOPE_AGENT);
      __hip_atomic_store((unsigned short*)(ob + 4096 + hj * 2), oe, __ATOMIC_RELAXED, __HIP_MEMORY_SCOPE_AGENT);
    }
    WAITVM(0);
    __syncthreads();
    if (tid == 0) __hip_atomic_fetch_add((unsigned*)(ws + WS_CNT + tgp * 128), 1u, __ATOMIC_RELAXED, __HIP_MEMORY_SCOPE_AGENT);
  }
  const char* ap; const char* bp; int astr; int n;
  if (w == 0) { ap = ws + WS_OF + (size_t)(mb * 4) * 20 * 1024; astr = 20 * 1024; bp = ws + WS_BF + (size_t)(nb * 74 + 54) * 5120; n = 20; }
  else { ap = ws + WS_HF + ((size_t)(mb * 4) * 54 + (w - 1) * 18) * 1024; astr = 54 * 1024; bp = ws + WS_BF + (size_t)(nb * 74 + (w - 1) * 18) * 5120; n = 18; }
  ap += l * 16; bp += l * 16;
  if (w == 0) {
    unsigned* cp = (unsigned*)(ws + WS_CNT + tgp * 128);
    int spins = 0;
    while (__hip_atomic_load(cp, __ATOMIC_RELAXED, __HIP_MEMORY_SCOPE_AGENT) < 32u && spins < (1 << 22)) { __builtin_amdgcn_s_sleep(2); ++spins; }
    __builtin_amdgcn_fence(__ATOMIC_ACQUIRE, "agent");
    WAITVM(0);
  }
  f32x4 acc[4][5];
#pragma unroll
  for (int i = 0; i < 4; ++i)
#pragma unroll
    for (int c = 0; c < 5; ++c) acc[i][c] = (f32x4)(0.f);
  u32x4 af[PF][4], bfr[PF][5];
#pragma unroll
  for (int k = 0; k < PF; ++k) {
#pragma unroll
    for (int c = 0; c < 5; ++c) bfr[k][c] = *(const u32x4*)(bp + k * 5120 + c * 1024);
#pragma unroll
    for (int i = 0; i < 4; ++i) af[k][i] = *(const u32x4*)(ap + (size_t)i * astr + k * 1024);
  }
#pragma unroll
  for (int k = 0; k < 20; ++k) {
    if (k < 18 || n == 20) {
#pragma unroll
      for (int i = 0; i < 4; ++i)
#pragma unroll
        for (int c = 0; c < 5; ++c) acc[i][c] = mfma32(bfr[k % PF][c], af[k % PF][i], acc[i][c]);
    }
    if (k + PF < 18 || (k + PF < 20 && n == 20)) {
#pragma unroll
      for (int c = 0; c < 5; ++c) bfr[k % PF][c] = *(const u32x4*)(bp + (k + PF) * 5120 + c * 1024);
#pragma unroll
      for (int i = 0; i < 4; ++i) af[k % PF][i] = *(const u32x4*)(ap + (size_t)i * astr + (k + PF) * 1024);
    }
  }
  const size_t orow = (size_t)(mb * 64 + w * 16 + lc) * 1280 + nb * 160 + g * 8;
  u32x2 xv[5];
#pragma unroll
  for (int c = 0; c < 5; ++c) xv[c] = *(const u32x2*)(x + orow + c * 32);
#pragma unroll
  for (int i = 0; i < 4; ++i)
#pragma unroll
    for (int c = 0; c < 5; ++c) *(LDSP(f32x4))(w * 20480 + (i * 5 + c) * 1024 + l * 16) = acc[i][c];
  __syncthreads();
#pragma unroll
  for (int c = 0; c < 5; ++c) {
    unsigned a = (w * 5 + c) * 1024 + l * 16;
    f32x4 sv = *(LDSP(f32x4))(a);
    f32x4 gv = (*(LDSP(f32x4))(a + 20480) + *(LDSP(f32x4))(a + 40960)) + *(LDSP(f32x4))(a + 61440);
    float o[4];
#pragma unroll
    for (int r = 0; r < 4; ++r) {
      float xf = __builtin_bit_cast(float, (r & 1) ? (xv[c][r >> 1] & 0xffff0000u) : (xv[c][r >> 1] << 16));
      o[r] = __builtin_fmaf(sv[r], silu_f(gv[r]), xf);
    }
    u32x2 ov = {pk2(o[0], o[1]), pk2(o[2], o[3])};
    *(u32x2*)(out + orow + c * 32) = ov;
  }
}

extern "C" void kernel_launch(void* const* d_in, const int* in_sizes, int n_in, void* d_out, int out_size, void* d_ws, size_t ws_size,
                              hipStream_t stream) {
  (void)in_sizes; (void)n_in; (void)out_size; (void)ws_size;
  hipFuncSetAttribute((const void*)k1, hipFuncAttributeMaxDynamicSharedMemorySize, K1_LDS);
  hipFuncSetAttribute((const void*)k2, hipFuncAttributeMaxDynamicSharedMemorySize, K2_LDS);
  k1<<<256, 512, K1_LDS, stream>>>((const char*)d_in[0], (const char*)d_in[1], (const char*)d_in[2], (const char*)d_in[3],
                                   (const char*)d_in[5], (const char*)d_in[6], (char*)d_ws);
  k2<<<256, 256, K2_LDS, stream>>>((const char*)d_in[0], (char*)d_ws, (char*)d_out);
}
```

```cpp
#include <hip/hip_runtime.h>
#include <stdint.h>

typedef __attribute__((ext_vector_type(8))) __bf16 bf16x8;
typedef __attribute__((ext_vector_type(2))) __bf16 bf16x2;
typedef __attribute__((ext_vector_type(4))) short s16x4;
typedef __attribute__((ext_vector_type(4))) float f32x4;
typedef __attribute__((ext_vector_type(2))) float f32x2;
typedef __attribute__((ext_vector_type(4))) unsigned u32x4;
typedef __attribute__((ext_vector_type(2))) unsigned u32x2;
#define LDSP(T) __attribute__((address_space(3))) T*
#define GLBP(T) __attribute__((address_space(1))) T*

constexpr size_t WS_HF  = 0;
constexpr size_t WS_OF  = WS_HF + 128ull * 54 * 1024;
constexpr size_t WS_OX  = WS_OF + 32ull * 128 * 512;
constexpr size_t WS_BF  = WS_OF + 128ull * 20 * 1024;
constexpr size_t WS_QF  = WS_BF + 8ull * 74 * 5 * 1024;
constexpr size_t WS_QX  = WS_QF + 32ull * 64 * 1024;
constexpr size_t WS_KVP = WS_QX + 8ull * 64 * 1024;
constexpr size_t WS_CNT = WS_KVP + 32ull * 16 * 3360;

#define STAMP(K, I)
constexpr int STG = 49152;
constexpr int L_WPHI = 3 * STG;
constexpr int L_CS = L_WPHI + 7168;
constexpr int L_ST = L_CS + 1024;
constexpr int K1_LDS = L_ST + 1024;
constexpr int RS = 144;
constexpr int KVI = 128 * RS;

__device__ __forceinline__ unsigned pk2(float a, float b) {
  f32x2 v = {a, b};
  bf16x2 r = __builtin_convertvector(v, bf16x2);
  return __builtin_bit_cast(unsigned, r);
}
__device__ __forceinline__ f32x4 mfma32(u32x4 a, u32x4 b, f32x4 c) {
  return __builtin_amdgcn_mfma_f32_16x16x32_bf16(__builtin_bit_cast(bf16x8, a), __builtin_bit_cast(bf16x8, b), c, 0, 0, 0);
}
__device__ __forceinline__ f32x4 mfma16(u32x2 a, u32x2 b, f32x4 c) {
  return __builtin_amdgcn_mfma_f32_16x16x16bf16_1k(__builtin_bit_cast(s16x4, a), __builtin_bit_cast(s16x4, b), c, 0, 0, 0);
}
__device__ __forceinline__ u32x2 trd(unsigned addr) {
  s16x4 v = __builtin_amdgcn_ds_read_tr16_b64_v4i16((LDSP(s16x4))addr);
  return __builtin_bit_cast(u32x2, v);
}
__device__ __forceinline__ u32x4 trd2(unsigned a0, unsigned a1) {
  u32x2 lo = trd(a0), hi = trd(a1);
  u32x4 r = {lo.x, lo.y, hi.x, hi.y};
  return r;
}
__device__ __forceinline__ u32x4 lds128(unsigned addr) { return *(LDSP(u32x4))addr; }
__device__ __forceinline__ void glds16(const char* g, unsigned ldsbase) {
  __builtin_amdgcn_global_load_lds((GLBP(const void))g, (LDSP(void))ldsbase, 16, 0, 0);
}
__device__ __forceinline__ float asf(unsigned u) { return __builtin_bit_cast(float, u); }
__device__ __forceinline__ unsigned asu(float f) { return __builtin_bit_cast(unsigned, f); }
__device__ __forceinline__ float gsum(float a) {
  auto r = __builtin_amdgcn_permlane16_swap(asu(a), asu(a), false, false);
  float m = asf(r[0]) + asf(r[1]);
  auto r2 = __builtin_amdgcn_permlane32_swap(asu(m), asu(m), false, false);
  return asf(r2[0]) + asf(r2[1]);
}
__device__ __forceinline__ float dot2(unsigned a, unsigned b, float c) {
  asm volatile("v_dot2c_f32_bf16 %0, %1, %2" : "+v"(c) : "v"(a), "v"(b));
  return c;
}
__device__ __forceinline__ float silu_f(float v) {
  float e = __builtin_amdgcn_exp2f(-1.4426950408889634f * v);
  return v * __builtin_amdgcn_rcpf(1.0f + e);
}
#define WAITVM(n) asm volatile("s_waitcnt vmcnt(" #n ")" ::: "memory")
#define WAITLGKM0() asm volatile("s_waitcnt lgkmcnt(0)" ::: "memory")
#define BAR() __builtin_amdgcn_s_barrier()

__device__ __forceinline__ void hedge(f32x4 p, float xe, float sc, u32x4& mainv, unsigned& extv) {
  float mx = fmaxf(fmaxf(fmaxf(p[0], p[1]), p[2]), fmaxf(p[3], xe));
  float mn = fminf(fminf(fminf(p[0], p[1]), p[2]), fminf(p[3], xe));
  auto r = __builtin_amdgcn_permlane16_swap(asu(mx), asu(-mn), false, false);
  float m = fmaxf(asf(r[0]), asf(r[1]));
  r = __builtin_amdgcn_permlane32_swap(asu(m), asu(m), false, false);
  m = fmaxf(asf(r[0]), asf(r[1])) * -sc;
  r = __builtin_amdgcn_permlane16_swap(asu(m), asu(m), false, false);
  const f32x2 bb = {asf(r[0]), asf(r[1])}, ss = {sc, -sc};
  f32x2 e[5];
#pragma unroll
  for (int i = 0; i < 5; ++i) {
    float v = i < 4 ? p[i & 3] : xe;
    f32x2 vv = {v, v};
    f32x2 t = __builtin_elementwise_fma(vv, ss, bb);
    e[i][0] = __builtin_amdgcn_exp2f(t[0]);
    e[i][1] = __builtin_amdgcn_exp2f(t[1]);
  }
  f32x2 s = ((e[0] + e[1]) + (e[2] + e[3])) + e[4];
  r = __builtin_amdgcn_permlane16_swap(asu(s[0]), asu(s[1]), false, false);
  m = asf(r[0]) + asf(r[1]);
  r = __builtin_amdgcn_permlane32_swap(asu(m), asu(m), false, false);
  m = __builtin_amdgcn_rcpf(asf(r[0]) + asf(r[1]));
  r = __builtin_amdgcn_permlane16_swap(asu(m), asu(m), false, false);
  const f32x2 rr = {asf(r[0]), asf(r[1])};
#pragma unroll
  for (int i = 0; i < 4; ++i) { f32x2 t = e[i] * rr; mainv[i] = pk2(t[0], t[1]); }
  f32x2 t = e[4] * rr;
  extv = pk2(t[0], t[1]);
}
__device__ __forceinline__ unsigned silu2(float u0, float u1, float c1, float irs) {
  f32x2 u = {u0, u1}, c = {c1, c1}, ir = {irs, irs};
  f32x2 t = u * c;
  f32x2 e = {__builtin_amdgcn_exp2f(t[0]), __builtin_amdgcn_exp2f(t[1])};
  f32x2 d = __builtin_elementwise_fma(e, ir, ir);
  f32x2 rc = {__builtin_amdgcn_rcpf(d[0]), __builtin_amdgcn_rcpf(d[1])};
  f32x2 h = u * rc;
  return pk2(h[0], h[1]);
}

template <int TYPE>
__device__ __forceinline__ void k1_body(const char* __restrict__ x, const char* __restrict__ wqkv, const char* __restrict__ wphi,
                                        const char* __restrict__ wo, const char* __restrict__ w1, const char* __restrict__ w2,
                                        char* __restrict__ ws, int rb, int jj, int bid) {
  constexpr int NT = TYPE == 2 ? 8 : 7;
  const int tid = threadIdx.x, l = tid & 63;
  const int w = __builtin_amdgcn_readfirstlane(tid >> 6);
  const int tg = w & 3, nh = w >> 2;
  const int g = l >> 4, lc = l & 15, q = (l >> 2) & 3, p = l & 3;
  STAMP(0, 0);
  const char* xp;
  {
    int row = 8 * w + (l >> 3);
    xp = x + (size_t)(rb * 128 + row) * 1280 + (((l & 7) ^ (l >> 3)) << 4);
  }
  const char* wp; int ld;
  {
    int row = 2 * w + (l >> 5);
    int sl = (l & 31) ^ (((row & 3) << 1) | (((row >> 3) & 1) << 3));
    if (TYPE == 0) {
      if (sl >= 24) { wp = w1 + jj * 128 + (sl - 24) * 16; ld = 3456; }
      else { int s2 = sl < 11 ? (sl > 9 ? 9 : sl) : (sl > 20 ? 9 : sl - 11);
             wp = wqkv + (sl < 11 ? 1280 : 2560) + jj * 160 + s2 * 16; ld = 3840; }
    } else if (TYPE == 1) {
      if (sl >= 24) { wp = w1 + (8 + jj) * 128 + (sl - 24) * 16; ld = 3456; }
      else { int s2 = sl > 19 ? 19 : sl; wp = wqkv + jj * 320 + s2 * 16; ld = 3840; }
    } else {
      int L = 12 + 4 * jj + (sl >> 3); L = L > 26 ? 26 : L;
      wp = w1 + L * 128 + (sl & 7) * 16; ld = 3456;
    }
    wp += (size_t)row * ld;
  }
  auto issue = [&](int s, int buf) {
    unsigned b = buf * STG;
    const char* xs = xp + s * 128;
    glds16(xs, b + w * 1024);
    glds16(xs + 64 * 1280, b + 8192 + w * 1024);
    const char* ws_ = wp + (size_t)(s * 64) * ld;
#pragma unroll
    for (int m = 0; m < 4; ++m) glds16(ws_ + (size_t)(16 * m) * ld, b + 16384 + (w + 8 * m) * 1024);
  };
  auto issue1 = [&](int s, int buf, int i) {
    unsigned b = buf * STG;
    if (i < 2) glds16(xp + s * 128 + i * 64 * 1280, b + i * 8192 + w * 1024);
    else glds16(wp + (size_t)(s * 64 + 16 * (i - 2)) * ld, b + 16384 + (w + 8 * (i - 2)) * 1024);
  };
  if (TYPE == 0) {
    constexpr int NCH = 200;
    if (w * 64 < NCH) {
      int c = tid < NCH ? tid : NCH - 1;
      glds16(wphi + (size_t)jj * (NCH * 16) + c * 16, L_WPHI + w * 1024);
    }
  }
  issue(0, 0); issue(1, 1); issue(2, 2);
  unsigned ta[NT], ca[2];
  {
    unsigned rowb = 16384 + (8 * g + q) * 512, sw = (q << 2) | ((g & 1) << 4);
    auto grp = [&](int t) -> int {
      if (TYPE == 0) {
        if (t < 2) return 5 * (2 * nh + t) + p;
        if (t == 2) return (p >> 1) * 22 + 10 * nh + 5 * (p & 1) + 4;
        if (t < 5) return 22 + 5 * (2 * nh + t - 3) + p;
        return 48 + 4 * (2 * nh + t - 5) + p;
      } else if (TYPE == 1) {
        if (t < 4) return 5 * (4 * nh + t) + p;
        if (t == 4) return 20 * nh + 5 * p + 4;
        return 48 + 4 * (2 * nh + t - 5) + p;
      } else return 32 * nh + 4 * t + p;
    };
#pragma unroll
    for (int t = 0; t < NT; ++t) ta[t] = rowb + ((grp(t) ^ sw) << 3);
#pragma unroll
    for (int c = 0; c < 2; ++c) {
      int t = 2 * tg + c; t = t >= NT ? NT - 1 : t;
      ca[c] = rowb + ((grp(t) ^ sw) << 3);
    }
  }
  unsigned xa[2][2];
#pragma unroll
  for (int tt = 0; tt < 2; ++tt)
#pragma unroll
    for (int k2 = 0; k2 < 2; ++k2) {
      int row = 32 * tg + 16 * (tt ^ nh) + lc;
      xa[tt][k2] = row * 128 + (((4 * k2 + g) ^ (row & 7)) << 4);
    }
  f32x4 acc[NT][2];
  float cs[2][2] = {{0.f, 0.f}, {0.f, 0.f}}, rsum[2] = {0.f, 0.f}, rsq[2] = {0.f, 0.f};
#pragma unroll
  for (int t = 0; t < NT; ++t) { acc[t][0] = (f32x4)(0.f); acc[t][1] = (f32x4)(0.f); }
  unsigned one2 = 0x3f803f80u;
  asm volatile("" : "+v"(one2));
  const int nside = (bid + 256 < 370) ? 2 : 1;
  auto side_issue = [&](int u, unsigned lb) {
    int ks = u / 5, cb = u - ks * 5;
    int v = 4 * w + (l >> 4);
    const char* src;
    if (ks < 54) src = w2 + (size_t)(32 * ks + v) * 1280;
    else {
      int k2 = ks - 54, hg = k2 / 5, kk = k2 - hg * 5, row;
      if (kk < 4) row = 20 * (8 * hg + 2 * kk + (v >> 4)) + (v & 15);
      else row = 20 * (8 * hg + (v & 3) + 4 * (v >> 4)) + 16 + ((v & 15) >> 2);
      src = wo + (size_t)row * 1280;
    }
    glds16(src + cb * 256 + (((l & 15) ^ ((v & 7) << 1)) << 4), lb + w * 1024);
  };

  constexpr int NF = NT + 2;
  unsigned fa[NF];
#pragma unroll
  for (int t = 0; t < NT; ++t) fa[t] = ta[t];
  fa[NT] = ca[0]; fa[NT + 1] = ca[1];
  u32x2 wl[2][NF], wh[2][NF];
  u32x4 xf[2][2];
#define RD(S, K2, I)                                                                                              \
  {                                                                                                               \
    if ((I) < 2) asm volatile("ds_read_b128 %0, %1" : "=v"(xf[S][(I) & 1]) : "v"(xa[(I) & 1][K2]));               \
    else if ((I) < 2 + 2 * NF) {                                                                                  \
      if (((I) & 1) == 0) asm volatile("ds_read_b64_tr_b16 %0, %1 offset:%2" : "=v"(wl[S][((I) - 2) >> 1 < NF ? ((I) - 2) >> 1 : 0]) : "v"(fa[((I) - 2) >> 1 < NF ? ((I) - 2) >> 1 : 0]), "i"((K2) * 16384)); \
      else asm volatile("ds_read_b64_tr_b16 %0, %1 offset:%2" : "=v"(wh[S][((I) - 2) >> 1 < NF ? ((I) - 2) >> 1 : 0]) : "v"(fa[((I) - 2) >> 1 < NF ? ((I) - 2) >> 1 : 0]), "i"((K2) * 16384 + 2048)); \
    }                                                                                                             \
  }
#define TIE(S)                                                                                                    \
  {                                                                                                               \
    asm volatile("" : "+v"(xf[S][0]), "+v"(xf[S][1]));                                                            \
    _Pragma("unroll") for (int t = 0; t < NF; ++t) asm volatile("" : "+v"(wl[S][t]), "+v"(wh[S][t]));             \
  }
#define SMODE 0
  f32x4 mcs[2] = {(f32x4)(0.f), (f32x4)(0.f)}, mrs = (f32x4)(0.f), mrq = (f32x4)(0.f);
  const u32x4 ones = {one2, one2, one2, one2};
#define UNP(V, LO, HI) asm volatile("v_lshlrev_b32 %0, 16, %2\n\tv_and_b32 %1, 0xffff0000, %2" : "=&v"(LO), "=v"(HI) : "v"(V));
#define STX(S, J) { float lo_, hi_; UNP(xf[S][0][J], lo_, hi_);                                                    \
    asm volatile("v_add_f32 %0, %0, %4\n\tv_add_f32 %1, %1, %5\n\tv_fmac_f32 %2, %4, %4\n\tv_fmac_f32 %3, %5, %5"  \
                 : "+v"(rsum[0]), "+v"(rsum[1]), "+v"(rsq[0]), "+v"(rsq[1]) : "v"(lo_), "v"(hi_)); }
#define STW(S, C, J) { float lo_, hi_; UNP(((J) & 2) ? wh[S][NT + (C)][(J) & 1] : wl[S][NT + (C)][(J) & 1], lo_, hi_); \
    asm volatile("v_add_f32 %0, %0, %2\n\tv_add_f32 %1, %1, %3" : "+v"(cs[C][0]), "+v"(cs[C][1]) : "v"(lo_), "v"(hi_)); }
#define WFR(S, T) ((u32x4){wl[S][T][0], wl[S][T][1], wh[S][T][0], wh[S][T][1]})
#define ST(S, I)                                                                                                  \
  {                                                                                                               \
    if (SMODE >= 1) { if ((I) < 12 && (I) % 3 == 0) STX(S, ((I) / 3) & 3); }                                      \
    else { if ((I) == 3) mrs = mfma32(ones, xf[S][0], mrs); if ((I) == 9) mrq = mfma32(xf[S][0], xf[S][0], mrq); } \
    if (SMODE == 2) { if ((I) < 12 && (I) % 3 != 0) STW(S, ((I) / 6) & 1, (((I) % 6) - 1 - ((I) % 6) / 3) & 3); } \
    else { if ((I) == 6) mcs[0] = mfma32(WFR(S, NT), ones, mcs[0]); if ((I) == 12) mcs[1] = mfma32(WFR(S, NT + 1), ones, mcs[1]); } \
  }
#define KSTEP(S, K2N, DORD, GL)                                                                                   \
  {                                                                                                               \
    _Pragma("unroll") for (int t = 0; t < NT; ++t) {                                                              \
      u32x4 wf = {wl[S][t][0], wl[S][t][1], wh[S][t][0], wh[S][t][1]};                                            \
      acc[t][0] = mfma32(wf, xf[S][0], acc[t][0]);                                                                \
      if (DORD) { RD((S) ^ 1, K2N, 4 * t); RD((S) ^ 1, K2N, 4 * t + 1); }                                         \
      ST(S, 2 * t);                                                            \
      __builtin_amdgcn_sched_barrier(0);                                                                          \
      acc[t][1] = mfma32(wf, xf[S][1], acc[t][1]);                                                                \
      if (DORD) { RD((S) ^ 1, K2N, 4 * t + 2); RD((S) ^ 1, K2N, 4 * t + 3); }                                     \
      ST(S, 2 * t + 1);                                                        \
      if ((GL) >= 0 && t >= 1 && t < 7) issue1(GL, (GL) % 3, t - 1);                                              \
      if ((GL) == -2 && t == 1) side_issue(bid, STG);                                                             \
      if ((GL) == -2 && t == 2) side_issue(nside == 2 ? bid + 256 : bid, STG + 8192);                             \
      __builtin_amdgcn_sched_barrier(0);                                                                          \
    }                                                                                                             \
  }
  WAITVM(12);
  BAR();
  STAMP(0, 1);
#pragma unroll
  for (int i = 0; i < 2 + 2 * NF; ++i) RD(0, 0, i);
#pragma unroll
  for (int s = 0; s < 10; ++s) {
    WAITLGKM0();
    TIE(0);
    __builtin_amdgcn_sched_barrier(0);
    KSTEP(0, 1, true, -1);
    WAITLGKM0();
    TIE(1);
    if (s < 9) {
      if (s < 8) WAITVM(6); else WAITVM(2);
      BAR();
      const int d = (s % 3 == 2) ? -2 * STG : STG;
#pragma unroll
      for (int t = 0; t < NF; ++t) fa[t] += d;
      xa[0][0] += d; xa[0][1] += d; xa[1][0] += d; xa[1][1] += d;
    }
    __builtin_amdgcn_sched_barrier(0);
    KSTEP(1, 0, s < 9, (s <= 6 ? s + 3 : (s == 7 ? -2 : -1)));
  }
  STAMP(0, 2);
  {
    if (SMODE == 2) {
      float c0 = gsum(cs[0][0] + cs[0][1]), c1 = gsum(cs[1][0] + cs[1][1]);
      if (g < 2) *(LDSP(float))(L_CS + nh * 512 + (2 * tg + g) * 64 + lc * 4) = g ? c1 : c0;
    } else if (lc < 2) *(LDSP(f32x4))(L_CS + nh * 512 + (2 * tg + lc) * 64 + g * 16) = lc ? mcs[1] : mcs[0];
    float sm, sq;
    if (SMODE >= 1) { sm = gsum(rsum[0] + rsum[1]); sq = gsum(rsq[0] + rsq[1]); }
    else { sm = mrs[0]; sq = p == 0 ? mrq[0] : (p == 1 ? mrq[1] : (p == 2 ? mrq[2] : mrq[3])); }
    float mean_ = sm * (1.0f / 640.0f);
    float var = fmaxf(sq * (1.0f / 640.0f) - mean_ * mean_, 0.f) + 1e-5f;
    f32x2 st = {mean_, __builtin_amdgcn_rsqf(var)};
    if (g == q) *(LDSP(f32x2))(L_ST + (32 * tg + 16 * nh + lc) * 8) = st;
  }
  WAITVM(0);
  WAITLGKM0();
  BAR();
  float mean[2], rstd[2];
#pragma unroll
  for (int tt = 0; tt < 2; ++tt) {
    f32x2 st = *(LDSP(f32x2))(L_ST + (32 * tg + 16 * (tt ^ nh) + lc) * 8);
    mean[tt] = st[0]; rstd[tt] = st[1];
  }
#pragma unroll
  for (int t = 0; t < NT; ++t) {
    f32x4 c4 = *(LDSP(f32x4))(L_CS + nh * 512 + t * 64 + g * 16);
#pragma unroll
    for (int tt = 0; tt < 2; ++tt) {
      f32x4 mm = {-mean[tt], -mean[tt], -mean[tt], -mean[tt]};
      acc[t][tt] = __builtin_elementwise_fma(mm, c4, acc[t][tt]);
    }
  }
  STAMP(0, 3);
  {
    unsigned a = STG + (4 * g + q) * 256 + (((2 * w + (p >> 1)) ^ (((4 * g + q) & 7) << 1)) << 4) + (p & 1) * 8;
#pragma unroll
    for (int i = 0; i < 2; ++i) {
      if (i < nside) {
        int u = bid + 256 * i, ks = u / 5, cb = u - ks * 5, ct = cb * 8 + w, nb = ct / 5, ci = ct - nb * 5;
        u32x4 f = trd2(a + i * 8192, a + i * 8192 + 4096);
        *(u32x4*)(ws + WS_BF + ((size_t)(nb * 74 + ks) * 5 + ci) * 1024 + l * 16) = f;
      }
    }
  }
  {
    constexpr int T0 = TYPE == 2 ? 0 : 5;
#pragma unroll
    for (int t = T0; t < NT; t += 2) {
      int ks;
      if (TYPE == 0) ks = 2 * jj + nh;
      else if (TYPE == 1) ks = 2 * (8 + jj) + nh;
      else ks = 2 * (12 + 4 * jj) + 4 * nh + (t >> 1);
      if (ks < 54) {
#pragma unroll
        for (int tt = 0; tt < 2; ++tt) {
          const float c1 = rstd[tt] * -1.4426950408889634f, irs = __builtin_amdgcn_rcpf(rstd[tt]);
          u32x4 o = {silu2(acc[t][tt][0], acc[t][tt][1], c1, irs), silu2(acc[t][tt][2], acc[t][tt][3], c1, irs),
                     silu2(acc[t + 1][tt][0], acc[t + 1][tt][1], c1, irs), silu2(acc[t + 1][tt][2], acc[t + 1][tt][3], c1, irs)};
          int gtt = rb * 8 + 2 * tg + (tt ^ nh);
          *(u32x4*)(ws + WS_HF + ((size_t)gtt * 54 + ks) * 1024 + l * 16) = o;
        }
      }
    }
  }
  STAMP(0, 4);
  if (TYPE == 2) return;
  if (TYPE == 1) {
    const int tp = rb * 4 + tg;
#pragma unroll
    for (int t = 0; t < 5; ++t) {
      f32x4 v0 = acc[t][0] * rstd[0], v1 = acc[t][1] * rstd[1];
      u32x2 a = {pk2(v0[0], v0[1]), pk2(v0[2], v0[3])}, b = {pk2(v1[0], v1[1]), pk2(v1[2], v1[3])};
      u32x4 o = nh ? (u32x4){b[0], b[1], a[0], a[1]} : (u32x4){a[0], a[1], b[0], b[1]};
      char* dst = t < 4 ? ws + WS_QF + ((size_t)(jj * 8 + nh * 4 + t) * 64 + tp) * 1024 : ws + WS_QX + ((size_t)(jj * 2 + nh) * 64 + tp) * 1024;
      *(u32x4*)(dst + l * 16) = o;
    }
    STAMP(0, 5);
    return;
  }
  u32x2 xb[2];
#pragma unroll
  for (int tt = 0; tt < 2; ++tt) { xb[tt][0] = pk2(acc[2][tt][0], acc[2][tt][1]); xb[tt][1] = pk2(acc[2][tt][2], acc[2][tt][3]); }
#pragma unroll
  for (int hh = 0; hh < 2; ++hh) {
    const int hl = 2 * nh + hh;
    unsigned wb = L_WPHI + hl * 800;
    u32x4 a0 = trd2(wb + (4 * g + q) * 40 + p * 8, wb + (16 + q) * 40 + p * 8);
    u32x4 a1;
    {
      unsigned b1 = wb + 4 * g * 40 + 32 + q * 2, b2 = wb + 640 + 32 + q * 2;
      a1[0] = (unsigned)*(LDSP(unsigned short))(b1) | ((unsigned)*(LDSP(unsigned short))(b1 + 40) << 16);
      a1[1] = (unsigned)*(LDSP(unsigned short))(b1 + 80) | ((unsigned)*(LDSP(unsigned short))(b1 + 120) << 16);
      a1[2] = (unsigned)*(LDSP(unsigned short))(b2) | ((unsigned)*(LDSP(unsigned short))(b2 + 40) << 16);
      a1[3] = (unsigned)*(LDSP(unsigned short))(b2 + 80) | ((unsigned)*(LDSP(unsigned short))(b2 + 120) << 16);
    }
    if (g != hh) { a0[2] = 0; a0[3] = 0; a1[2] = 0; a1[3] = 0; }
#pragma unroll
    for (int tt = 0; tt < 2; ++tt) {
      u32x4 kb = {pk2(acc[hh][tt][0], acc[hh][tt][1]), pk2(acc[hh][tt][2], acc[hh][tt][3]), xb[tt][0], xb[tt][1]};
      f32x4 pm = mfma32(a0, kb, (f32x4)(0.f));
      f32x4 px = mfma32(a1, kb, (f32x4)(0.f));
      u32x4 mv; unsigned ev;
      hedge(pm, px[0], rstd[tt] * 1.4426950408889634f, mv, ev);
      unsigned ib = (nh ? 0 : 2 * STG) + hh * KVI + (32 * tg + 16 * (tt ^ nh) + lc) * RS;
      *(LDSP(u32x4))(ib + g * 16) = mv;
      *(LDSP(unsigned))(ib + 64 + g * 4) = ev;
      f32x4 v = acc[3 + hh][tt] * rstd[tt];
      u32x2 vv = {pk2(v[0], v[1]), pk2(v[2], v[3])};
      *(LDSP(u32x2))(ib + 80 + g * 8) = vv;
    }
  }
  STAMP(0, 5);
  {
#pragma unroll
    for (int tt = 0; tt < 2; ++tt) {
      unsigned ib = (nh ? 0 : 2 * STG) + (g & 1) * KVI + (32 * tg + 16 * (tt ^ nh) + lc) * RS + (g >= 2 ? 112 : 120);
      f32x4 v = acc[2][tt] * rstd[tt];
      u32x2 vv = {pk2(v[0], v[1]), pk2(v[2], v[3])};
      if (g < 2) { vv[0] = 0x00003f80u; vv[1] = 0u; }
      *(LDSP(u32x2))(ib) = vv;
    }
    WAITLGKM0();
    BAR();
    const int hl = w >> 1, dt = w & 1;
    unsigned ib = (hl < 2 ? 2 * STG : 0) + (hl & 1) * KVI + (4 * g + q) * RS + p * 8;
    f32x4 kv0 = (f32x4)(0.f), kv1 = (f32x4)(0.f), kv2 = (f32x4)(0.f);
#pragma unroll
    for (int k = 0; k < 4; ++k) {
      unsigned a = ib + k * 32 * RS;
      u32x4 bv = trd2(a + 80 + dt * 32, a + 80 + dt * 32 + 16 * RS);
      kv0 = mfma32(trd2(a, a + 16 * RS), bv, kv0);
      kv1 = mfma32(trd2(a + 32, a + 32 + 16 * RS), bv, kv1);
      kv2 = mfma32(trd2(a + 64, a + 64 + 16 * RS), bv, kv2);
    }
    if (dt == 0 || lc < 5) {
      int col = 16 * dt + lc;
      char* o = ws + WS_KVP + ((size_t)(jj * 4 + hl) * 16 + rb) * 3360 + col * 160 + g * 16;
      *(f32x4*)(o) = kv0;
      *(f32x4*)(o + 64) = kv1;
      if (g < 2) *(f32x4*)(o + 128) = kv2;
    }
    STAMP(0, 6);
  }
}

__global__ __launch_bounds__(512, 2) void k1(const char* __restrict__ x, const char* __restrict__ wqkv, const char* __restrict__ wphi,
                                             const char* __restrict__ wo, const char* __restrict__ w1, const char* __restrict__ w2,
                                             char* __restrict__ ws) {
  const int bid = blockIdx.x, xcd = bid & 7, idx = bid >> 3;
  const int rb = (xcd >> 2) * 8 + (idx >> 2), j = xcd & 3, ty = idx & 3;
  if (bid == 0 && threadIdx.x < 8) *(unsigned*)(ws + WS_CNT + threadIdx.x * 128) = 0u;
  if (ty < 2) k1_body<0>(x, wqkv, wphi, wo, w1, w2, ws, rb, 2 * j + ty, bid);
  else if (ty == 2) k1_body<1>(x, wqkv, wphi, wo, w1, w2, ws, rb, j, bid);
  else k1_body<2>(x, wqkv, wphi, wo, w1, w2, ws, rb, j, bid);
}

constexpr int L2_G = 102400;
constexpr int L2_S = 97280;
constexpr int K2_LDS = 163840;
#define PF 4
__device__ __forceinline__ void lds_wait(LDSP(unsigned) f, unsigned v) {
  for (int sp = 0; sp < (1 << 22); ++sp) {
    if (__hip_atomic_load(f, __ATOMIC_RELAXED, __HIP_MEMORY_SCOPE_WORKGROUP) >= v) break;
    __builtin_amdgcn_s_sleep(1);
  }
  asm volatile("" ::: "memory");
}
__global__ __launch_bounds__(448, 2) void k2(const char* __restrict__ x, const char* __restrict__ wphi, char* __restrict__ ws, char* __restrict__ out) {
  const int bid = blockIdx.x, tgp = bid & 7, idx = bid >> 3;
  const int mb = 4 * tgp + (idx & 3), nb = idx >> 2;
  const int tid = threadIdx.x, l = tid & 63;
  const int w = __builtin_amdgcn_readfirstlane(tid >> 6);
  const int g = l >> 4, lc = l & 15;
  const unsigned lo = l * 16;
  STAMP(1, 0);
  LDSP(unsigned) flg = (LDSP(unsigned))(L2_S + 2048);
  if (w < 4) {
    __builtin_amdgcn_s_setprio(3);
    const int head = idx;
    u32x4 qf[2], qx[2];
#pragma unroll
    for (int i = 0; i < 2; ++i) {
      int tp = tgp * 8 + w * 2 + i;
      qf[i] = *(const u32x4*)(ws + WS_QF + ((size_t)head * 64 + tp) * 1024 + lo);
      qx[i] = *(const u32x4*)(ws + WS_QX + ((size_t)(head >> 2) * 64 + tp) * 1024 + lo);
    }
    const unsigned short* wq = (const unsigned short*)(wphi + head * 800);
    unsigned short wv[16];
#pragma unroll
    for (int j = 0; j < 4; ++j) {
      wv[j] = wq[(4 * g + j) * 20 + lc]; wv[4 + j] = wq[(16 + j) * 20 + lc];
      wv[8 + j] = wq[(4 * g + j) * 20 + 16 + (lc >> 2)]; wv[12 + j] = wq[(16 + j) * 20 + 16 + (lc >> 2)];
    }
    f32x4 part[16];
    const char* kp = ws + WS_KVP + (size_t)head * 16 * 3360 + (tid < 210 ? tid : 209) * 16;
#pragma unroll
    for (int i = 0; i < 16; ++i) part[i] = *(const f32x4*)(kp + i * 3360);
    if (tid < 4) flg[tid] = 0u;
    __builtin_amdgcn_sched_barrier(0);
    WAITLGKM0();
    BAR();
    __builtin_amdgcn_sched_barrier(0);
    u32x4 a0, a1;
#pragma unroll
    for (int j = 0; j < 4; ++j) { a0[j] = (unsigned)wv[2 * j] | ((unsigned)wv[2 * j + 1] << 16); a1[j] = (unsigned)wv[8 + 2 * j] | ((unsigned)wv[9 + 2 * j] << 16); }
    if (g != (head & 3)) { a0[2] = 0; a0[3] = 0; a1[2] = 0; a1[3] = 0; }
    u32x4 pq[4]; unsigned px[4];
#pragma unroll
    for (int i = 0; i < 4; ++i) {
      u32x4 kb = {qf[i >> 1][2 * (i & 1)], qf[i >> 1][2 * (i & 1) + 1], qx[i >> 1][2 * (i & 1)], qx[i >> 1][2 * (i & 1) + 1]};
      f32x4 pm = mfma32(a0, kb, (f32x4)(0.f));
      f32x4 pe = mfma32(a1, kb, (f32x4)(0.f));
      hedge(pm, pe[0], 1.4426950408889634f, pq[i], px[i]);
    }
    __builtin_amdgcn_sched_barrier(0);
    f32x4 s = ((part[0] + part[1]) + (part[2] + part[3])) + ((part[4] + part[5]) + (part[6] + part[7])) +
              (((part[8] + part[9]) + (part[10] + part[11])) + ((part[12] + part[13]) + (part[14] + part[15])));
    u32x2 sb = {pk2(s[0], s[1]), pk2(s[2], s[3])};
    if (tid < 210) *(LDSP(u32x2))(L2_S + tid * 8) = sb;
    WAITLGKM0();
    if (l == 0) __hip_atomic_fetch_add(flg, 1u, __ATOMIC_RELAXED, __HIP_MEMORY_SCOPE_WORKGROUP);
    lds_wait(flg, 4u);
    STAMP(1, 1);
    int c1 = (lc & 3) == 0 ? 16 + (lc >> 2) : 20;
    u32x4 b0 = lds128(L2_S + lc * 80 + g * 16), b1 = lds128(L2_S + c1 * 80 + g * 16);
    u32x4 e0 = {*(LDSP(unsigned))(L2_S + lc * 80 + 64 + g * 4), 0u, 0u, 0u}, e1 = {*(LDSP(unsigned))(L2_S + c1 * 80 + 64 + g * 4), 0u, 0u, 0u};
    float oe[4];
#pragma unroll
    for (int i = 0; i < 4; ++i) {
      u32x4 pe = {px[i], 0u, 0u, 0u};
      f32x4 d0 = mfma32(b0, pq[i], (f32x4)(0.f));
      f32x4 d1 = mfma32(b1, pq[i], (f32x4)(0.f));
      d0 = mfma32(e0, pe, d0);
      d1 = mfma32(e1, pe, d1);
      float inv = __builtin_amdgcn_rcpf(d1[1] + 1e-6f);
      unsigned long long ov = (unsigned long long)pk2(d0[0] * inv, d0[1] * inv) | ((unsigned long long)pk2(d0[2] * inv, d0[3] * inv) << 32);
      oe[i] = d1[0] * inv;
      int gtt = tgp * 16 + w * 4 + i;
      __hip_atomic_store((unsigned long long*)(ws + WS_OF + ((size_t)head * 128 + gtt) * 512 + l * 8), ov, __ATOMIC_RELAXED, __HIP_MEMORY_SCOPE_AGENT);
    }
    {
      unsigned long long ov = (unsigned long long)pk2(oe[0], oe[1]) | ((unsigned long long)pk2(oe[2], oe[3]) << 32);
      __hip_atomic_store((unsigned long long*)(ws + WS_OX + ((size_t)head * 32 + tgp * 4 + w) * 512 + l * 8), ov, __ATOMIC_RELAXED, __HIP_MEMORY_SCOPE_AGENT);
    }
    WAITVM(0);
    STAMP(1, 2);
    if (l == 0) {
      unsigned old = __hip_atomic_fetch_add(flg + 1, 1u, __ATOMIC_RELAXED, __HIP_MEMORY_SCOPE_WORKGROUP);
      if (old == 3u) __hip_atomic_fetch_add((unsigned*)(ws + WS_CNT + tgp * 128), 1u, __ATOMIC_RELAXED, __HIP_MEMORY_SCOPE_AGENT);
    }
    const char* bp = ws + WS_BF + (size_t)(nb * 74 + 54) * 5120 + lo;
    u32x4 bl[5];
#pragma unroll
    for (int c = 0; c < 5; ++c) bl[c] = *(const u32x4*)(bp + (95 + c) * 1024);
    const size_t orow = (size_t)(mb * 64 + w * 16 + lc) * 1280 + nb * 160 + g * 8;
    u32x2 xv[5];
#pragma unroll
    for (int c = 0; c < 5; ++c) xv[c] = *(const u32x2*)(x + orow + c * 32);
    if (w == 0) {
      unsigned* cp = (unsigned*)(ws + WS_CNT + tgp * 128);
      int spins = 0;
      while (__hip_atomic_load(cp, __ATOMIC_RELAXED, __HIP_MEMORY_SCOPE_AGENT) < 32u && spins < (1 << 22)) { __builtin_amdgcn_s_sleep(1); ++spins; }
      __builtin_amdgcn_fence(__ATOMIC_ACQUIRE, "agent");
      WAITVM(0);
      if (l == 0) __hip_atomic_store(flg + 2, 1u, __ATOMIC_RELAXED, __HIP_MEMORY_SCOPE_WORKGROUP);
    } else {
      for (int i = w - 1; i < 95; i += 3) glds16(bp + i * 1024, i * 1024);
      WAITVM(0);
      if (l == 0) __hip_atomic_fetch_add(flg + 3, 1u, __ATOMIC_RELAXED, __HIP_MEMORY_SCOPE_WORKGROUP);
      lds_wait(flg + 2, 1u);
    }
    STAMP(1, 3);
    u32x2 om[32]; unsigned short ox[32];
    {
      const char* op = ws + WS_OF + (size_t)(mb * 4 + w) * 512 + l * 8;
      const char* oxp = ws + WS_OX + (size_t)mb * 512 + l * 8 + w * 2;
#pragma unroll
      for (int h = 0; h < 32; ++h) {
        om[h] = *(const u32x2*)(op + (size_t)h * 65536);
        ox[h] = *(const unsigned short*)(oxp + (size_t)h * 16384);
      }
    }
    __builtin_amdgcn_sched_barrier(0);
    lds_wait(flg + 3, 3u);
    f32x4 acc[5];
#pragma unroll
    for (int c = 0; c < 5; ++c) acc[c] = (f32x4)(0.f);
#pragma unroll
    for (int k = 0; k < 20; ++k) {
      const int hg = k / 5, kk = k % 5;
      u32x4 af;
      if (kk < 4) af = (u32x4){om[8 * hg + 2 * kk][0], om[8 * hg + 2 * kk][1], om[8 * hg + 2 * kk + 1][0], om[8 * hg + 2 * kk + 1][1]};
      else {
#pragma unroll
        for (int j = 0; j < 4; ++j) af[j] = (unsigned)ox[8 * hg + 2 * j] | ((unsigned)ox[8 * hg + 2 * j + 1] << 16);
      }
#pragma unroll
      for (int c = 0; c < 5; ++c) acc[c] = mfma32(k < 19 ? lds128((k * 5 + c) * 1024 + lo) : bl[c], af, acc[c]);
    }
    STAMP(1, 4);
    BAR();
#pragma unroll
    for (int c = 0; c < 5; ++c) {
      unsigned a = L2_G + (w * 5 + c) * 1024 + lo;
      f32x4 gv = (*(LDSP(f32x4))(a) + *(LDSP(f32x4))(a + 20480)) + *(LDSP(f32x4))(a + 40960);
      float o[4];
#pragma unroll
      for (int r = 0; r < 4; ++r) {
        float xf = __builtin_bit_cast(float, (r & 1) ? (xv[c][r >> 1] & 0xffff0000u) : (xv[c][r >> 1] << 16));
        o[r] = __builtin_fmaf(acc[c][r], silu_f(gv[r]), xf);
      }
      u32x2 ov = {pk2(o[0], o[1]), pk2(o[2], o[3])};
      *(u32x2*)(out + orow + c * 32) = ov;
    }
    STAMP(1, 5);
  } else {
    f32x4 acc[4][5];
#pragma unroll
    for (int i = 0; i < 4; ++i)
#pragma unroll
      for (int c = 0; c < 5; ++c) acc[i][c] = (f32x4)(0.f);
    const char* ap = ws + WS_HF + ((size_t)(mb * 4) * 54 + (w - 4) * 18) * 1024;
    const char* bp = ws + WS_BF + (size_t)(nb * 74 + (w - 4) * 18) * 5120;
    u32x4 af[PF][4], bfr[PF][5];
    BAR();
#pragma unroll
    for (int k = 0; k < PF; ++k) {
#pragma unroll
      for (int c = 0; c < 5; ++c) bfr[k][c] = *(const u32x4*)(bp + (k * 5120 + c * 1024) + lo);
#pragma unroll
      for (int i = 0; i < 4; ++i) af[k][i] = *(const u32x4*)(ap + (i * 55296 + k * 1024) + lo);
    }
    __builtin_amdgcn_sched_barrier(0);
#pragma unroll
    for (int k = 0; k < 18; ++k) {
#pragma unroll
      for (int i = 0; i < 4; ++i)
#pragma unroll
        for (int c = 0; c < 5; ++c) acc[i][c] = mfma32(bfr[k % PF][c], af[k % PF][i], acc[i][c]);
      __builtin_amdgcn_sched_barrier(0);
      if (k + PF < 18) {
#pragma unroll
        for (int c = 0; c < 5; ++c) bfr[k % PF][c] = *(const u32x4*)(bp + ((k + PF) * 5120 + c * 1024) + lo);
#pragma unroll
        for (int i = 0; i < 4; ++i) af[k % PF][i] = *(const u32x4*)(ap + (i * 55296 + (k + PF) * 1024) + lo);
      }
      __builtin_amdgcn_sched_barrier(0);
    }
    STAMP(1, 4);
    unsigned base = L2_G + (w - 4) * 20480 + lo;
#pragma unroll
    for (int i = 0; i < 4; ++i)
#pragma unroll
      for (int c = 0; c < 5; ++c) *(LDSP(f32x4))(base + (i * 5 + c) * 1024) = acc[i][c];
    WAITLGKM0();
    BAR();
  }
}

extern "C" void kernel_launch(void* const* d_in, const int* in_sizes, int n_in, void* d_out, int out_size, void* d_ws, size_t ws_size,
                              hipStream_t stream) {
  (void)in_sizes; (void)n_in; (void)out_size; (void)ws_size;
  hipFuncSetAttribute((const void*)k1, hipFuncAttributeMaxDynamicSharedMemorySize, K1_LDS);
  hipFuncSetAttribute((const void*)k2, hipFuncAttributeMaxDynamicSharedMemorySize, K2_LDS);
  k1<<<256, 512, K1_LDS, stream>>>((const char*)d_in[0], (const char*)d_in[1], (const char*)d_in[2], (const char*)d_in[3],
                                   (const char*)d_in[5], (const char*)d_in[6], (char*)d_ws);
  k2<<<256, 448, K2_LDS, stream>>>((const char*)d_in[0], (const char*)d_in[2], (char*)d_ws, (char*)d_out);
}
```

```cpp
#include <hip/hip_runtime.h>
#include <stdint.h>

typedef __attribute__((ext_vector_type(8))) __bf16 bf16x8;
typedef __attribute__((ext_vector_type(2))) __bf16 bf16x2;
typedef __attribute__((ext_vector_type(4))) short s16x4;
typedef __attribute__((ext_vector_type(4))) float f32x4;
typedef __attribute__((ext_vector_type(2))) float f32x2;
typedef __attribute__((ext_vector_type(4))) unsigned u32x4;
typedef __attribute__((ext_vector_type(2))) unsigned u32x2;
#define LDSP(T) __attribute__((address_space(3))) T*
#define GLBP(T) __attribute__((address_space(1))) T*

constexpr size_t WS_HF  = 0;
constexpr size_t WS_OF  = WS_HF + 128ull * 54 * 1024;
constexpr size_t WS_OX  = WS_OF + 32ull * 128 * 512;
constexpr size_t WS_BF  = WS_OF + 128ull * 20 * 1024;
constexpr size_t WS_QF  = WS_BF + 8ull * 74 * 5 * 1024;
constexpr size_t WS_QX  = WS_QF + 32ull * 64 * 1024;
constexpr size_t WS_KVP = WS_QX + 8ull * 64 * 1024;
constexpr size_t WS_WF  = WS_KVP + 32ull * 16 * 3360;
constexpr size_t WS_CNT = WS_WF + 32ull * 2048;

#define STAMP(K, I)
constexpr int STG = 49152;
constexpr int L_WPHI = 3 * STG;
constexpr int L_CS = L_WPHI + 7168;
constexpr int L_ST = L_CS + 1024;
constexpr int K1_LDS = L_ST + 1024;
constexpr int RS = 144;
constexpr int KVI = 128 * RS;

__device__ __forceinline__ unsigned pk2(float a, float b) {
  f32x2 v = {a, b};
  bf16x2 r = __builtin_convertvector(v, bf16x2);
  return __builtin_bit_cast(unsigned, r);
}
__device__ __forceinline__ f32x4 mfma32(u32x4 a, u32x4 b, f32x4 c) {
  return __builtin_amdgcn_mfma_f32_16x16x32_bf16(__builtin_bit_cast(bf16x8, a), __builtin_bit_cast(bf16x8, b), c, 0, 0, 0);
}
__device__ __forceinline__ f32x4 mfma16(u32x2 a, u32x2 b, f32x4 c) {
  return __builtin_amdgcn_mfma_f32_16x16x16bf16_1k(__builtin_bit_cast(s16x4, a), __builtin_bit_cast(s16x4, b), c, 0, 0, 0);
}
__device__ __forceinline__ u32x2 trd(unsigned addr) {
  s16x4 v = __builtin_amdgcn_ds_read_tr16_b64_v4i16((LDSP(s16x4))addr);
  return __builtin_bit_cast(u32x2, v);
}
__device__ __forceinline__ u32x4 trd2(unsigned a0, unsigned a1) {
  u32x2 lo = trd(a0), hi = trd(a1);
  u32x4 r = {lo.x, lo.y, hi.x, hi.y};
  return r;
}
__device__ __forceinline__ u32x4 lds128(unsigned addr) { return *(LDSP(u32x4))addr; }
__device__ __forceinline__ void glds16(const char* g, unsigned ldsbase) {
  __builtin_amdgcn_global_load_lds((GLBP(const void))g, (LDSP(void))ldsbase, 16, 0, 0);
}
__device__ __forceinline__ float asf(unsigned u) { return __builtin_bit_cast(float, u); }
__device__ __forceinline__ unsigned asu(float f) { return __builtin_bit_cast(unsigned, f); }
__device__ __forceinline__ float gsum(float a) {
  auto r = __builtin_amdgcn_permlane16_swap(asu(a), asu(a), false, false);
  float m = asf(r[0]) + asf(r[1]);
  auto r2 = __builtin_amdgcn_permlane32_swap(asu(m), asu(m), false, false);
  return asf(r2[0]) + asf(r2[1]);
}
__device__ __forceinline__ float dot2(unsigned a, unsigned b, float c) {
  asm volatile("v_dot2c_f32_bf16 %0, %1, %2" : "+v"(c) : "v"(a), "v"(b));
  return c;
}
__device__ __forceinline__ float silu_f(float v) {
  float e = __builtin_amdgcn_exp2f(-1.4426950408889634f * v);
  return v * __builtin_amdgcn_rcpf(1.0f + e);
}
#define WAITVM_(n) asm volatile("s_waitcnt vmcnt(" #n ")" ::: "memory")
#define WAITVM(n) WAITVM_(n)
#define WAITLGKM0() asm volatile("s_waitcnt lgkmcnt(0)" ::: "memory")
#define BAR() __builtin_amdgcn_s_barrier()

__device__ __forceinline__ void hedge(f32x4 p, float xe, float sc, u32x4& mainv, unsigned& extv) {
  float mx = fmaxf(fmaxf(fmaxf(p[0], p[1]), p[2]), fmaxf(p[3], xe));
  float mn = fminf(fminf(fminf(p[0], p[1]), p[2]), fminf(p[3], xe));
  auto r = __builtin_amdgcn_permlane16_swap(asu(mx), asu(-mn), false, false);
  float m = fmaxf(asf(r[0]), asf(r[1]));
  r = __builtin_amdgcn_permlane32_swap(asu(m), asu(m), false, false);
  m = fmaxf(asf(r[0]), asf(r[1])) * -sc;
  r = __builtin_amdgcn_permlane16_swap(asu(m), asu(m), false, false);
  const f32x2 bb = {asf(r[0]), asf(r[1])}, ss = {sc, -sc};
  f32x2 e[5];
#pragma unroll
  for (int i = 0; i < 5; ++i) {
    float v = i < 4 ? p[i & 3] : xe;
    f32x2 vv = {v, v};
    f32x2 t = __builtin_elementwise_fma(vv, ss, bb);
    e[i][0] = __builtin_amdgcn_exp2f(t[0]);
    e[i][1] = __builtin_amdgcn_exp2f(t[1]);
  }
  f32x2 s = ((e[0] + e[1]) + (e[2] + e[3])) + e[4];
  r = __builtin_amdgcn_permlane16_swap(asu(s[0]), asu(s[1]), false, false);
  m = asf(r[0]) + asf(r[1]);
  r = __builtin_amdgcn_permlane32_swap(asu(m), asu(m), false, false);
  m = __builtin_amdgcn_rcpf(asf(r[0]) + asf(r[1]));
  r = __builtin_amdgcn_permlane16_swap(asu(m), asu(m), false, false);
  const f32x2 rr = {asf(r[0]), asf(r[1])};
#pragma unroll
  for (int i = 0; i < 4; ++i) { f32x2 t = e[i] * rr; mainv[i] = pk2(t[0], t[1]); }
  f32x2 t = e[4] * rr;
  extv = pk2(t[0], t[1]);
}
__device__ __forceinline__ unsigned silu2(float u0, float u1, float c1, float irs) {
  f32x2 u = {u0, u1}, c = {c1, c1}, ir = {irs, irs};
  f32x2 t = u * c;
  f32x2 e = {__builtin_amdgcn_exp2f(t[0]), __builtin_amdgcn_exp2f(t[1])};
  f32x2 d = __builtin_elementwise_fma(e, ir, ir);
  f32x2 rc = {__builtin_amdgcn_rcpf(d[0]), __builtin_amdgcn_rcpf(d[1])};
  f32x2 h = u * rc;
  return pk2(h[0], h[1]);
}

template <int TYPE>
__device__ __forceinline__ void k1_body(const char* __restrict__ x, const char* __restrict__ wqkv, const char* __restrict__ wphi,
                                        const char* __restrict__ wo, const char* __restrict__ w1, const char* __restrict__ w2,
                                        char* __restrict__ ws, int rb, int jj, int bid) {
  constexpr int NT = TYPE == 2 ? 8 : 7;
  const int tid = threadIdx.x, l = tid & 63;
  const int w = __builtin_amdgcn_readfirstlane(tid >> 6);
  const int tg = w & 3, nh = w >> 2;
  const int g = l >> 4, lc = l & 15, q = (l >> 2) & 3, p = l & 3;
  STAMP(0, 0);
  const char* xp;
  {
    int row = 8 * w + (l >> 3);
    xp = x + (size_t)(rb * 128 + row) * 1280 + (((l & 7) ^ (l >> 3)) << 4);
  }
  const char* wp; int ld;
  {
    int row = 2 * w + (l >> 5);
    int sl = (l & 31) ^ (((row & 3) << 1) | (((row >> 3) & 1) << 3));
    if (TYPE == 0) {
      if (sl >= 24) { wp = w1 + jj * 128 + (sl - 24) * 16; ld = 3456; }
      else { int s2 = sl < 11 ? (sl > 9 ? 9 : sl) : (sl > 20 ? 9 : sl - 11);
             wp = wqkv + (sl < 11 ? 1280 : 2560) + jj * 160 + s2 * 16; ld = 3840; }
    } else if (TYPE == 1) {
      if (sl >= 24) { wp = w1 + (8 + jj) * 128 + (sl - 24) * 16; ld = 3456; }
      else { int s2 = sl > 19 ? 19 : sl; wp = wqkv + jj * 320 + s2 * 16; ld = 3840; }
    } else {
      int L = 12 + 4 * jj + (sl >> 3); L = L > 26 ? 26 : L;
      wp = w1 + L * 128 + (sl & 7) * 16; ld = 3456;
    }
    wp += (size_t)row * ld;
  }
  auto issue = [&](int s, int buf) {
    unsigned b = buf * STG;
    const char* xs = xp + s * 128;
    glds16(xs, b + w * 1024);
    glds16(xs + 64 * 1280, b + 8192 + w * 1024);
    const char* ws_ = wp + (size_t)(s * 64) * ld;
#pragma unroll
    for (int m = 0; m < 4; ++m) glds16(ws_ + (size_t)(16 * m) * ld, b + 16384 + (w + 8 * m) * 1024);
  };
  auto issue1 = [&](int s, int buf, int i) {
    unsigned b = buf * STG;
    if (i < 2) glds16(xp + s * 128 + i * 64 * 1280, b + i * 8192 + w * 1024);
    else glds16(wp + (size_t)(s * 64 + 16 * (i - 2)) * ld, b + 16384 + (w + 8 * (i - 2)) * 1024);
  };
  if (TYPE == 0) {
    constexpr int NCH = 200;
    if (w * 64 < NCH) {
      int c = tid < NCH ? tid : NCH - 1;
      glds16(wphi + (size_t)jj * (NCH * 16) + c * 16, L_WPHI + w * 1024);
    }
  }
  issue(0, 0); issue(1, 1); issue(2, 2);
  unsigned ta[NT], ca[2];
  {
    unsigned rowb = 16384 + (8 * g + q) * 512, sw = (q << 2) | ((g & 1) << 4);
    auto grp = [&](int t) -> int {
      if (TYPE == 0) {
        if (t < 2) return 5 * (2 * nh + t) + p;
        if (t == 2) return (p >> 1) * 22 + 10 * nh + 5 * (p & 1) + 4;
        if (t < 5) return 22 + 5 * (2 * nh + t - 3) + p;
        return 48 + 4 * (2 * nh + t - 5) + p;
      } else if (TYPE == 1) {
        if (t < 4) return 5 * (4 * nh + t) + p;
        if (t == 4) return 20 * nh + 5 * p + 4;
        return 48 + 4 * (2 * nh + t - 5) + p;
      } else return 32 * nh + 4 * t + p;
    };
#pragma unroll
    for (int t = 0; t < NT; ++t) ta[t] = rowb + ((grp(t) ^ sw) << 3);
#pragma unroll
    for (int c = 0; c < 2; ++c) {
      int t = 2 * tg + c; t = t >= NT ? NT - 1 : t;
      ca[c] = rowb + ((grp(t) ^ sw) << 3);
    }
  }
  unsigned xa[2][2];
#pragma unroll
  for (int tt = 0; tt < 2; ++tt)
#pragma unroll
    for (int k2 = 0; k2 < 2; ++k2) {
      int row = 32 * tg + 16 * (tt ^ nh) + lc;
      xa[tt][k2] = row * 128 + (((4 * k2 + g) ^ (row & 7)) << 4);
    }
  f32x4 acc[NT][2];
  float cs[2][2] = {{0.f, 0.f}, {0.f, 0.f}}, rsum[2] = {0.f, 0.f}, rsq[2] = {0.f, 0.f};
#pragma unroll
  for (int t = 0; t < NT; ++t) { acc[t][0] = (f32x4)(0.f); acc[t][1] = (f32x4)(0.f); }
  unsigned one2 = 0x3f803f80u;
  asm volatile("" : "+v"(one2));
  const int nside = (bid + 256 < 370) ? 2 : 1;
  auto side_issue = [&](int u, unsigned lb) {
    int ks = u / 5, cb = u - ks * 5;
    int v = 4 * w + (l >> 4);
    const char* src;
    if (ks < 54) src = w2 + (size_t)(32 * ks + v) * 1280;
    else {
      int k2 = ks - 54, hg = k2 / 5, kk = k2 - hg * 5, row;
      if (kk < 4) row = 20 * (8 * hg + 2 * kk + (v >> 4)) + (v & 15);
      else row = 20 * (8 * hg + 2 * ((v & 15) >> 2) + (v >> 4)) + 16 + (v & 3);
      src = wo + (size_t)row * 1280;
    }
    glds16(src + cb * 256 + (((l & 15) ^ ((v & 7) << 1)) << 4), lb + w * 1024);
  };

  constexpr int NF = NT + 2;
  unsigned fa[NF];
#pragma unroll
  for (int t = 0; t < NT; ++t) fa[t] = ta[t];
  fa[NT] = ca[0]; fa[NT + 1] = ca[1];
  u32x2 wl[2][NF], wh[2][NF];
  u32x4 xf[2][2];
#define RD(S, K2, I)                                                                                              \
  {                                                                                                               \
    if ((I) < 2) asm volatile("ds_read_b128 %0, %1" : "=v"(xf[S][(I) & 1]) : "v"(xa[(I) & 1][K2]));               \
    else if ((I) < 2 + 2 * NF) {                                                                                  \
      if (((I) & 1) == 0) asm volatile("ds_read_b64_tr_b16 %0, %1 offset:%2" : "=v"(wl[S][((I) - 2) >> 1 < NF ? ((I) - 2) >> 1 : 0]) : "v"(fa[((I) - 2) >> 1 < NF ? ((I) - 2) >> 1 : 0]), "i"((K2) * 16384)); \
      else asm volatile("ds_read_b64_tr_b16 %0, %1 offset:%2" : "=v"(wh[S][((I) - 2) >> 1 < NF ? ((I) - 2) >> 1 : 0]) : "v"(fa[((I) - 2) >> 1 < NF ? ((I) - 2) >> 1 : 0]), "i"((K2) * 16384 + 2048)); \
    }                                                                                                             \
  }
#define TIE(S)                                                                                                    \
  {                                                                                                               \
    asm volatile("" : "+v"(xf[S][0]), "+v"(xf[S][1]));                                                            \
    _Pragma("unroll") for (int t = 0; t < NF; ++t) asm volatile("" : "+v"(wl[S][t]), "+v"(wh[S][t]));             \
  }
#define SMODE 0
  f32x4 mcs[2] = {(f32x4)(0.f), (f32x4)(0.f)}, mrs = (f32x4)(0.f), mrq = (f32x4)(0.f);
  const u32x4 ones = {one2, one2, one2, one2};
#define UNP(V, LO, HI) asm volatile("v_lshlrev_b32 %0, 16, %2\n\tv_and_b32 %1, 0xffff0000, %2" : "=&v"(LO), "=v"(HI) : "v"(V));
#define STX(S, J) { float lo_, hi_; UNP(xf[S][0][J], lo_, hi_);                                                    \
    asm volatile("v_add_f32 %0, %0, %4\n\tv_add_f32 %1, %1, %5\n\tv_fmac_f32 %2, %4, %4\n\tv_fmac_f32 %3, %5, %5"  \
                 : "+v"(rsum[0]), "+v"(rsum[1]), "+v"(rsq[0]), "+v"(rsq[1]) : "v"(lo_), "v"(hi_)); }
#define STW(S, C, J) { float lo_, hi_; UNP(((J) & 2) ? wh[S][NT + (C)][(J) & 1] : wl[S][NT + (C)][(J) & 1], lo_, hi_); \
    asm volatile("v_add_f32 %0, %0, %2\n\tv_add_f32 %1, %1, %3" : "+v"(cs[C][0]), "+v"(cs[C][1]) : "v"(lo_), "v"(hi_)); }
#define WFR(S, T) ((u32x4){wl[S][T][0], wl[S][T][1], wh[S][T][0], wh[S][T][1]})
#define ST(S, I)                                                                                                  \
  {                                                                                                               \
    if (SMODE >= 1) { if ((I) < 12 && (I) % 3 == 0) STX(S, ((I) / 3) & 3); }                                      \
    else { if ((I) == 3) mrs = mfma32(ones, xf[S][0], mrs); if ((I) == 9) mrq = mfma32(xf[S][0], xf[S][0], mrq); } \
    if (SMODE == 2) { if ((I) < 12 && (I) % 3 != 0) STW(S, ((I) / 6) & 1, (((I) % 6) - 1 - ((I) % 6) / 3) & 3); } \
    else { if ((I) == 6) mcs[0] = mfma32(WFR(S, NT), ones, mcs[0]); if ((I) == 12) mcs[1] = mfma32(WFR(S, NT + 1), ones, mcs[1]); } \
  }
#define KSTEP(S, K2N, DORD, GL)                                                                                   \
  {                                                                                                               \
    _Pragma("unroll") for (int t = 0; t < NT; ++t) {                                                              \
      u32x4 wf = {wl[S][t][0], wl[S][t][1], wh[S][t][0], wh[S][t][1]};                                            \
      acc[t][0] = mfma32(wf, xf[S][0], acc[t][0]);                                                                \
      if (DORD) { RD((S) ^ 1, K2N, 4 * t); RD((S) ^ 1, K2N, 4 * t + 1); }                                         \
      ST(S, 2 * t);                                                            \
      __builtin_amdgcn_sched_barrier(0);                                                                          \
      acc[t][1] = mfma32(wf, xf[S][1], acc[t][1]);                                                                \
      if (DORD) { RD((S) ^ 1, K2N, 4 * t + 2); RD((S) ^ 1, K2N, 4 * t + 3); }                                     \
      ST(S, 2 * t + 1);                                                        \
      if ((GL) >= 0 && t >= 1 && t < 7) issue1(GL, (GL) % 3, t - 1);                                              \
      if ((GL) == -2 && t == 1) side_issue(bid, STG);                                                             \
      if ((GL) == -2 && t == 2) side_issue(nside == 2 ? bid + 256 : bid, STG + 8192);                             \
      __builtin_amdgcn_sched_barrier(0);                                                                          \
    }                                                                                                             \
  }
  WAITVM(12);
  BAR();
  STAMP(0, 1);
#pragma unroll
  for (int i = 0; i < 2 + 2 * NF; ++i) RD(0, 0, i);
#pragma unroll
  for (int s = 0; s < 10; ++s) {
    WAITLGKM0();
    TIE(0);
    __builtin_amdgcn_sched_barrier(0);
    KSTEP(0, 1, true, -1);
    WAITLGKM0();
    TIE(1);
    if (s < 9) {
      if (s < 8) WAITVM(6); else WAITVM(2);
      BAR();
      const int d = (s % 3 == 2) ? -2 * STG : STG;
#pragma unroll
      for (int t = 0; t < NF; ++t) fa[t] += d;
      xa[0][0] += d; xa[0][1] += d; xa[1][0] += d; xa[1][1] += d;
    }
    __builtin_amdgcn_sched_barrier(0);
    KSTEP(1, 0, s < 9, (s <= 6 ? s + 3 : (s == 7 ? -2 : -1)));
  }
  STAMP(0, 2);
  {
    if (SMODE == 2) {
      float c0 = gsum(cs[0][0] + cs[0][1]), c1 = gsum(cs[1][0] + cs[1][1]);
      if (g < 2) *(LDSP(float))(L_CS + nh * 512 + (2 * tg + g) * 64 + lc * 4) = g ? c1 : c0;
    } else if (lc < 2) *(LDSP(f32x4))(L_CS + nh * 512 + (2 * tg + lc) * 64 + g * 16) = lc ? mcs[1] : mcs[0];
    float sm, sq;
    if (SMODE >= 1) { sm = gsum(rsum[0] + rsum[1]); sq = gsum(rsq[0] + rsq[1]); }
    else { sm = mrs[0]; sq = p == 0 ? mrq[0] : (p == 1 ? mrq[1] : (p == 2 ? mrq[2] : mrq[3])); }
    float mean_ = sm * (1.0f / 640.0f);
    float var = fmaxf(sq * (1.0f / 640.0f) - mean_ * mean_, 0.f) + 1e-5f;
    f32x2 st = {mean_, __builtin_amdgcn_rsqf(var)};
    if (g == q) *(LDSP(f32x2))(L_ST + (32 * tg + 16 * nh + lc) * 8) = st;
  }
  WAITVM(0);
  WAITLGKM0();
  BAR();
  float mean[2], rstd[2];
#pragma unroll
  for (int tt = 0; tt < 2; ++tt) {
    f32x2 st = *(LDSP(f32x2))(L_ST + (32 * tg + 16 * (tt ^ nh) + lc) * 8);
    mean[tt] = st[0]; rstd[tt] = st[1];
  }
#pragma unroll
  for (int t = 0; t < NT; ++t) {
    f32x4 c4 = *(LDSP(f32x4))(L_CS + nh * 512 + t * 64 + g * 16);
#pragma unroll
    for (int tt = 0; tt < 2; ++tt) {
      f32x4 mm = {-mean[tt], -mean[tt], -mean[tt], -mean[tt]};
      acc[t][tt] = __builtin_elementwise_fma(mm, c4, acc[t][tt]);
    }
  }
  STAMP(0, 3);
  {
    unsigned a = STG + (4 * g + q) * 256 + (((2 * w + (p >> 1)) ^ (((4 * g + q) & 7) << 1)) << 4) + (p & 1) * 8;
#pragma unroll
    for (int i = 0; i < 2; ++i) {
      if (i < nside) {
        int u = bid + 256 * i, ks = u / 5, cb = u - ks * 5, ct = cb * 8 + w, nb = ct / 5, ci = ct - nb * 5;
        u32x4 f = trd2(a + i * 8192, a + i * 8192 + 4096);
        *(u32x4*)(ws + WS_BF + ((size_t)(nb * 74 + ks) * 5 + ci) * 1024 + l * 16) = f;
      }
    }
  }
  {
    constexpr int T0 = TYPE == 2 ? 0 : 5;
#pragma unroll
    for (int t = T0; t < NT; t += 2) {
      int ks;
      if (TYPE == 0) ks = 2 * jj + nh;
      else if (TYPE == 1) ks = 2 * (8 + jj) + nh;
      else ks = 2 * (12 + 4 * jj) + 4 * nh + (t >> 1);
      if (ks < 54) {
#pragma unroll
        for (int tt = 0; tt < 2; ++tt) {
          const float c1 = rstd[tt] * -1.4426950408889634f, irs = __builtin_amdgcn_rcpf(rstd[tt]);
          u32x4 o = {silu2(acc[t][tt][0], acc[t][tt][1], c1, irs), silu2(acc[t][tt][2], acc[t][tt][3], c1, irs),
                     silu2(acc[t + 1][tt][0], acc[t + 1][tt][1], c1, irs), silu2(acc[t + 1][tt][2], acc[t + 1][tt][3], c1, irs)};
          int gtt = rb * 8 + 2 * tg + (tt ^ nh);
          *(u32x4*)(ws + WS_HF + ((size_t)gtt * 54 + ks) * 1024 + l * 16) = o;
        }
      }
    }
  }
  STAMP(0, 4);
  if (TYPE == 2) return;
  if (TYPE == 1) {
    const int tp = rb * 4 + tg;
#pragma unroll
    for (int t = 0; t < 5; ++t) {
      f32x4 v0 = acc[t][0] * rstd[0], v1 = acc[t][1] * rstd[1];
      u32x2 a = {pk2(v0[0], v0[1]), pk2(v0[2], v0[3])}, b = {pk2(v1[0], v1[1]), pk2(v1[2], v1[3])};
      u32x4 o = nh ? (u32x4){b[0], b[1], a[0], a[1]} : (u32x4){a[0], a[1], b[0], b[1]};
      char* dst = t < 4 ? ws + WS_QF + ((size_t)(jj * 8 + nh * 4 + t) * 64 + tp) * 1024 : ws + WS_QX + ((size_t)(jj * 2 + nh) * 64 + tp) * 1024;
      *(u32x4*)(dst + l * 16) = o;
    }
    {
      const u32x4 sen = {0xffffffffu, 0xffffffffu, 0xffffffffu, 0xffffffffu};
      char* dst = ws + WS_OF + (size_t)(rb * 4 + jj) * 40960 + tid * 16;
#pragma unroll
      for (int i = 0; i < 5; ++i) *(u32x4*)(dst + i * 8192) = sen;
    }
    STAMP(0, 5);
    return;
  }
  u32x2 xb[2];
#pragma unroll
  for (int tt = 0; tt < 2; ++tt) { xb[tt][0] = pk2(acc[2][tt][0], acc[2][tt][1]); xb[tt][1] = pk2(acc[2][tt][2], acc[2][tt][3]); }
#pragma unroll
  for (int hh = 0; hh < 2; ++hh) {
    const int hl = 2 * nh + hh;
    unsigned wb = L_WPHI + hl * 800;
    u32x4 a0 = trd2(wb + (4 * g + q) * 40 + p * 8, wb + (16 + q) * 40 + p * 8);
    u32x4 a1;
    {
      unsigned b1 = wb + 4 * g * 40 + 32 + q * 2, b2 = wb + 640 + 32 + q * 2;
      a1[0] = (unsigned)*(LDSP(unsigned short))(b1) | ((unsigned)*(LDSP(unsigned short))(b1 + 40) << 16);
      a1[1] = (unsigned)*(LDSP(unsigned short))(b1 + 80) | ((unsigned)*(LDSP(unsigned short))(b1 + 120) << 16);
      a1[2] = (unsigned)*(LDSP(unsigned short))(b2) | ((unsigned)*(LDSP(unsigned short))(b2 + 40) << 16);
      a1[3] = (unsigned)*(LDSP(unsigned short))(b2 + 80) | ((unsigned)*(LDSP(unsigned short))(b2 + 120) << 16);
    }
    if (rb == 0 && tg == 0) {
      char* wf = ws + WS_WF + (size_t)(jj * 4 + hl) * 2048 + l * 16;
      *(u32x4*)wf = a0; *(u32x4*)(wf + 1024) = a1;
    }
    if (g != hh) { a0[2] = 0; a0[3] = 0; a1[2] = 0; a1[3] = 0; }
#pragma unroll
    for (int tt = 0; tt < 2; ++tt) {
      u32x4 kb = {pk2(acc[hh][tt][0], acc[hh][tt][1]), pk2(acc[hh][tt][2], acc[hh][tt][3]), xb[tt][0], xb[tt][1]};
      f32x4 pm = mfma32(a0, kb, (f32x4)(0.f));
      f32x4 px = mfma32(a1, kb, (f32x4)(0.f));
      u32x4 mv; unsigned ev;
      hedge(pm, px[0], rstd[tt] * 1.4426950408889634f, mv, ev);
      unsigned ib = (nh ? 0 : 2 * STG) + hh * KVI + (32 * tg + 16 * (tt ^ nh) + lc) * RS;
      *(LDSP(u32x4))(ib + g * 16) = mv;
      *(LDSP(unsigned))(ib + 64 + g * 4) = ev;
      f32x4 v = acc[3 + hh][tt] * rstd[tt];
      u32x2 vv = {pk2(v[0], v[1]), pk2(v[2], v[3])};
      *(LDSP(u32x2))(ib + 80 + g * 8) = vv;
    }
  }
  STAMP(0, 5);
  {
#pragma unroll
    for (int tt = 0; tt < 2; ++tt) {
      unsigned ib = (nh ? 0 : 2 * STG) + (g & 1) * KVI + (32 * tg + 16 * (tt ^ nh) + lc) * RS + (g >= 2 ? 112 : 120);
      f32x4 v = acc[2][tt] * rstd[tt];
      u32x2 vv = {pk2(v[0], v[1]), pk2(v[2], v[3])};
      if (g < 2) { vv[0] = 0x00003f80u; vv[1] = 0u; }
      *(LDSP(u32x2))(ib) = vv;
    }
    WAITLGKM0();
    BAR();
    const int hl = w >> 1, dt = w & 1;
    unsigned ib = (hl < 2 ? 2 * STG : 0) + (hl & 1) * KVI + (4 * g + q) * RS + p * 8;
    f32x4 kv0 = (f32x4)(0.f), kv1 = (f32x4)(0.f), kv2 = (f32x4)(0.f);
#pragma unroll
    for (int k = 0; k < 4; ++k) {
      unsigned a = ib + k * 32 * RS;
      u32x4 bv = trd2(a + 80 + dt * 32, a + 80 + dt * 32 + 16 * RS);
      kv0 = mfma32(trd2(a, a + 16 * RS), bv, kv0);
      kv1 = mfma32(trd2(a + 32, a + 32 + 16 * RS), bv, kv1);
      kv2 = mfma32(trd2(a + 64, a + 64 + 16 * RS), bv, kv2);
    }
    if (dt == 0 || lc < 5) {
      int col = 16 * dt + lc;
      char* o = ws + WS_KVP + ((size_t)(jj * 4 + hl) * 16 + rb) * 3360 + col * 160 + g * 16;
      *(f32x4*)(o) = kv0;
      *(f32x4*)(o + 64) = kv1;
      if (g < 2) *(f32x4*)(o + 128) = kv2;
    }
    STAMP(0, 6);
  }
}

__global__ __launch_bounds__(512, 2) void k1(const char* __restrict__ x, const char* __restrict__ wqkv, const char* __restrict__ wphi,
                                             const char* __restrict__ wo, const char* __restrict__ w1, const char* __restrict__ w2,
                                             char* __restrict__ ws) {
  const int bid = blockIdx.x, xcd = bid & 7, idx = bid >> 3;
  const int rb = (xcd >> 2) * 8 + (idx >> 2), j = xcd & 3, ty = idx & 3;
  if (ty < 2) k1_body<0>(x, wqkv, wphi, wo, w1, w2, ws, rb, 2 * j + ty, bid);
  else if (ty == 2) k1_body<1>(x, wqkv, wphi, wo, w1, w2, ws, rb, j, bid);
  else k1_body<2>(x, wqkv, wphi, wo, w1, w2, ws, rb, j, bid);
}

constexpr int L2_G = 102400;
constexpr int L2_S = 97280;
constexpr int K2_LDS = 163840;
#define PF 3
#define HP 2
#define SLP2 4
#define WOD 58
#define WB 90
#define PRE 0
#define PRE0 6
#define SLP3 2
#define GB 0
#define AUX 0
#define ROT 0
#define ROTX 0
#define MAP 1
#define SLP0 8
#define SLP1 4
__device__ __forceinline__ void lds_wait(LDSP(unsigned) f, unsigned v) {
  for (int sp = 0; sp < (1 << 22); ++sp) {
    if (__hip_atomic_load(f, __ATOMIC_RELAXED, __HIP_MEMORY_SCOPE_WORKGROUP) >= v) break;
    __builtin_amdgcn_s_sleep(1);
  }
  asm volatile("" ::: "memory");
}
__global__ __launch_bounds__(512, 2) void k2(const char* __restrict__ x, const char* __restrict__ wphi, char* __restrict__ ws, char* __restrict__ out) {
  const int bid = blockIdx.x, xcd = bid & 7, idx = bid >> 3;
  const int mb = 8 * (xcd >> 1) + (idx & 7), nb = 4 * (xcd & 1) + (idx >> 3);
  const int tgp = idx >> 2;
  const int tid = threadIdx.x, l = tid & 63;
  const int w = __builtin_amdgcn_readfirstlane(tid >> 6);
  const int g = l >> 4, lc = l & 15;
  const unsigned lo = l * 16;
  STAMP(1, 0);
  LDSP(unsigned) flg = (LDSP(unsigned))(L2_S + 2048);
  if (w < 4) {
    __builtin_amdgcn_s_setprio(HP);
    const int head = xcd * 4 + (idx & 3);
    u32x4 qf[2], qx[2];
#pragma unroll
    for (int i = 0; i < 2; ++i) {
      int tp = tgp * 8 + w * 2 + i;
      qf[i] = *(const u32x4*)(ws + WS_QF + ((size_t)head * 64 + tp) * 1024 + lo);
      qx[i] = *(const u32x4*)(ws + WS_QX + ((size_t)(head >> 2) * 64 + tp) * 1024 + lo);
    }
    u32x4 a0 = *(const u32x4*)(ws + WS_WF + (size_t)head * 2048 + lo), a1 = *(const u32x4*)(ws + WS_WF + (size_t)head * 2048 + 1024 + lo);
    f32x4 part[16];
    const char* kp = ws + WS_KVP + (size_t)head * 16 * 3360 + (tid < 210 ? tid : 209) * 16;
#pragma unroll
    for (int i = 0; i < 16; ++i) part[i] = *(const f32x4*)(kp + i * 3360);
    if (tid < 4) flg[tid] = 0u;
    __builtin_amdgcn_sched_barrier(0);
    WAITLGKM0();
    BAR();
    __builtin_amdgcn_sched_barrier(0);
    if (g != (head & 3)) { a0[2] = 0; a0[3] = 0; a1[2] = 0; a1[3] = 0; }
    u32x4 pq[4]; unsigned px[4];
#pragma unroll
    for (int i = 0; i < 4; ++i) {
      u32x4 kb = {qf[i >> 1][2 * (i & 1)], qf[i >> 1][2 * (i & 1) + 1], qx[i >> 1][2 * (i & 1)], qx[i >> 1][2 * (i & 1) + 1]};
      f32x4 pm = mfma32(a0, kb, (f32x4)(0.f));
      f32x4 pe = mfma32(a1, kb, (f32x4)(0.f));
      hedge(pm, pe[0], 1.4426950408889634f, pq[i], px[i]);
    }
    __builtin_amdgcn_sched_barrier(0);
    f32x4 s = ((part[0] + part[1]) + (part[2] + part[3])) + ((part[4] + part[5]) + (part[6] + part[7])) +
              (((part[8] + part[9]) + (part[10] + part[11])) + ((part[12] + part[13]) + (part[14] + part[15])));
    u32x2 sb = {pk2(s[0], s[1]), pk2(s[2], s[3])};
    if (tid < 210) *(LDSP(u32x2))(L2_S + tid * 8) = sb;
    WAITLGKM0();
    if (l == 0) __hip_atomic_fetch_add(flg, 1u, __ATOMIC_RELAXED, __HIP_MEMORY_SCOPE_WORKGROUP);
    lds_wait(flg, 4u);
    STAMP(1, 1);
    {
      const int c1 = 16 + (lc & 3);
      u32x4 b0 = lds128(L2_S + lc * 80 + g * 16), b1 = lds128(L2_S + c1 * 80 + g * 16), b2 = lds128(L2_S + 1600 + g * 16);
      u32x4 e0 = {*(LDSP(unsigned))(L2_S + lc * 80 + 64 + g * 4), 0u, 0u, 0u}, e1 = {*(LDSP(unsigned))(L2_S + c1 * 80 + 64 + g * 4), 0u, 0u, 0u},
            e2 = {*(LDSP(unsigned))(L2_S + 1600 + 64 + g * 4), 0u, 0u, 0u};
      unsigned long long oe = 0;
#pragma unroll
      for (int i = 0; i < 4; ++i) {
        u32x4 pe = {px[i], 0u, 0u, 0u};
        f32x4 d2 = mfma32(b2, pq[i], (f32x4)(0.f));
        f32x4 d0 = mfma32(b0, pq[i], (f32x4)(0.f));
        f32x4 d1 = mfma32(b1, pq[i], (f32x4)(0.f));
        d2 = mfma32(e2, pe, d2);
        d0 = mfma32(e0, pe, d0);
        d1 = mfma32(e1, pe, d1);
        float inv = __builtin_amdgcn_rcpf(d2[0] + 1e-6f);
        unsigned long long ov = (unsigned long long)pk2(d0[0] * inv, d0[1] * inv) | ((unsigned long long)pk2(d0[2] * inv, d0[3] * inv) << 32);
        unsigned long long ev = (unsigned long long)pk2(d1[0] * inv, d1[1] * inv) | ((unsigned long long)pk2(d1[2] * inv, d1[3] * inv) << 32);
        if (g == i) oe = ev;
        int gtt = tgp * 16 + w * 4 + i;
        __hip_atomic_store((unsigned long long*)(ws + WS_OF + ((size_t)head * 128 + gtt) * 512 + l * 8), ov, __ATOMIC_RELAXED, __HIP_MEMORY_SCOPE_AGENT);
      }
      __hip_atomic_store((unsigned long long*)(ws + WS_OX + ((size_t)((tgp * 16 + w * 4 + g) * 4 + (head >> 3)) * 2 + (head & 1)) * 512 + ((head & 7) >> 1) * 128 + lc * 8),
                         oe, __ATOMIC_RELAXED, __HIP_MEMORY_SCOPE_AGENT);
    }
    STAMP(1, 2);
    __builtin_amdgcn_sched_barrier(0);
    const char* bp = ws + WS_BF + (size_t)(nb * 74 + 54) * 5120 + lo;
    u32x4 bl[5];
#pragma unroll
    for (int c = 0; c < 5; ++c) bl[c] = *(const u32x4*)(bp + (95 + c) * 1024);
    const size_t orow = (size_t)(mb * 64 + w * 16 + lc) * 1280 + nb * 160 + g * 8;
    u32x2 xv[5];
#pragma unroll
    for (int c = 0; c < 5; ++c) xv[c] = *(const u32x2*)(x + orow + c * 32);
    __builtin_amdgcn_sched_barrier(0);
    unsigned long long om[32], ox[8];
    {
      const char* op = ws + WS_OF + (size_t)(mb * 4 + w) * 512 + l * 8;
      const char* oxp = ws + WS_OX + (size_t)(mb * 4 + w) * 4096 + l * 8;
      unsigned need = 15u;
      __builtin_amdgcn_s_sleep(SLP0);
      for (int sp = 0; sp < (1 << 16); ++sp) {
#pragma unroll
        for (int hg = 0; hg < 4; ++hg)
          if (need & (1u << hg)) {
#pragma unroll
            for (int j = 0; j < 8; ++j)
              om[8 * hg + j] = __hip_atomic_load((const unsigned long long*)(op + (size_t)(8 * hg + j) * 65536), __ATOMIC_RELAXED, __HIP_MEMORY_SCOPE_AGENT);
#pragma unroll
            for (int j = 0; j < 2; ++j)
              ox[2 * hg + j] = __hip_atomic_load((const unsigned long long*)(oxp + (2 * hg + j) * 512), __ATOMIC_RELAXED, __HIP_MEMORY_SCOPE_AGENT);
          }
#pragma unroll
        for (int hg = 0; hg < 4; ++hg)
          if (need & (1u << hg)) {
            unsigned m = max(max((unsigned)ox[2 * hg], (unsigned)(ox[2 * hg] >> 32)), max((unsigned)ox[2 * hg + 1], (unsigned)(ox[2 * hg + 1] >> 32)));
#pragma unroll
            for (int j = 0; j < 8; ++j) m = max(max(m, (unsigned)om[8 * hg + j]), (unsigned)(om[8 * hg + j] >> 32));
            if (__builtin_amdgcn_ballot_w64(m == 0xffffffffu) == 0ull) need &= ~(1u << hg);
          }
        if (need == 0u) break;
        __builtin_amdgcn_s_sleep(SLP1);
      }
    }
    STAMP(1, 3);
    __builtin_amdgcn_sched_barrier(0);
    lds_wait(flg + 3, 4u);
    u32x4 wf[3][5];
#pragma unroll
    for (int k = 0; k < 3; ++k)
#pragma unroll
      for (int c = 0; c < 5; ++c) wf[k][c] = lds128((k * 5 + c) * 1024 + lo);
    STAMP(1, 6);
    f32x4 acc[5];
#pragma unroll
    for (int c = 0; c < 5; ++c) acc[c] = (f32x4)(0.f);
#pragma unroll
    for (int k = 0; k < 20; ++k) {
      const int hg = k / 5, kk = k % 5;
      const unsigned long long f0 = kk < 4 ? om[8 * hg + 2 * (kk & 3)] : ox[2 * hg], f1 = kk < 4 ? om[8 * hg + 2 * (kk & 3) + 1] : ox[2 * hg + 1];
      u32x4 af = {(unsigned)f0, (unsigned)(f0 >> 32), (unsigned)f1, (unsigned)(f1 >> 32)};
#pragma unroll
      for (int c = 0; c < 5; ++c) {
        acc[c] = mfma32(k < 19 ? wf[k % 3][c] : bl[c], af, acc[c]);
        if (k + 3 < 19) wf[k % 3][c] = lds128(((k + 3) * 5 + c) * 1024 + lo);
        __builtin_amdgcn_sched_barrier(0);
      }
    }
    __builtin_amdgcn_s_setprio(0);
    if (l == 0) __hip_atomic_fetch_add(flg + 2, 1u, __ATOMIC_RELAXED, __HIP_MEMORY_SCOPE_WORKGROUP);
    STAMP(1, 4);
    BAR();
#pragma unroll
    for (int c = 0; c < 5; ++c) {
      unsigned a = L2_G + (w * 5 + c) * 1024 + lo;
      f32x4 gv = (*(LDSP(f32x4))(a) + *(LDSP(f32x4))(a + 20480)) + (*(LDSP(f32x4))(a + 40960) + *(LDSP(f32x4))(a - L2_G));
      float o[4];
#pragma unroll
      for (int r = 0; r < 4; ++r) {
        float xf = __builtin_bit_cast(float, (r & 1) ? (xv[c][r >> 1] & 0xffff0000u) : (xv[c][r >> 1] << 16));
        o[r] = __builtin_fmaf(acc[c][r], silu_f(gv[r]), xf);
      }
      u32x2 ov = {pk2(o[0], o[1]), pk2(o[2], o[3])};
      *(u32x2*)(out + orow + c * 32) = ov;
    }
    STAMP(1, 5);
  } else {
    f32x4 acc[4][5];
#pragma unroll
    for (int i = 0; i < 4; ++i)
#pragma unroll
      for (int c = 0; c < 5; ++c) acc[i][c] = (f32x4)(0.f);
    const int k0 = (w - 4) * 14 - (w == 7 ? 1 : 0);
    const int nk = w < 6 ? 14 : 13;
    const int rot = ROT ? (2 * (idx & 7) + 7 * ((idx >> 3) & 1) + 3 * (idx >> 4) + ROTX * xcd) % 13 : 0;
    auto kidx = [&](int k) { int kn = (k < nk ? k : nk - 1) + rot; return kn >= nk ? kn - nk : kn; };
    const __amdgpu_buffer_rsrc_t rs = __builtin_amdgcn_make_buffer_rsrc(ws, 0, (int)WS_CNT, 0x00020000);
    const int ap = (int)WS_HF + (mb * 4 * 54 + k0) * 1024, bp = (int)WS_BF + (nb * 74 + k0) * 5120;
#define BLD(OFF) __builtin_bit_cast(u32x4, __builtin_amdgcn_raw_buffer_load_b128(rs, lo, (OFF), AUX))
    u32x4 af[PF][4], bfr[PF][5];
    const int f0 = w == 7 ? 71 : (w - 4) * 24;
    const char* wp = ws + WS_BF + (size_t)(nb * 74 + 54) * 5120 + f0 * 1024 + lo;
    if (GB == 0) BAR();
#pragma unroll
    for (int i0 = 0; i0 < 6; ++i0) {
      const int i = ROT ? (i0 + (idx & 7)) % 6 : i0;
      const char* a = wp + i * 4096;
      asm volatile("s_mov_b32 m0, %1\n\ts_nop 0\n\tglobal_load_lds_dwordx4 %0, off\n\tglobal_load_lds_dwordx4 %0, off offset:1024\n\t"
                   "global_load_lds_dwordx4 %0, off offset:2048\n\tglobal_load_lds_dwordx4 %0, off offset:3072"
                   ::"v"(a), "s"((f0 + 4 * i) * 1024) : "memory");
    }
    if (GB == 1) BAR();
#pragma unroll
    for (int k = 0; k < PF; ++k) {
      const int kn = kidx(k);
#pragma unroll
      for (int c = 0; c < 5; ++c) bfr[k][c] = BLD(bp + kn * 5120 + c * 1024);
#pragma unroll
      for (int i = 0; i < 4; ++i) af[k][i] = BLD(ap + i * 55296 + kn * 1024);
    }
    __builtin_amdgcn_sched_barrier(0);
#pragma unroll
    for (int k = 0; k < 14; ++k) {
      if (k == 13 && w >= 6) {
#pragma unroll
        for (int i = 0; i < 4; ++i) af[k % PF][i] = (u32x4)(0u);
      }
#pragma unroll
      for (int i = 0; i < 4; ++i)
#pragma unroll
        for (int c = 0; c < 5; ++c) acc[i][c] = mfma32(bfr[k % PF][c], af[k % PF][i], acc[i][c]);
      __builtin_amdgcn_sched_barrier(0);
      if (k == 0 && l == 0) __hip_atomic_fetch_add(flg + 3, 1u, __ATOMIC_RELAXED, __HIP_MEMORY_SCOPE_WORKGROUP);
      if (k + PF < 14) {
        const int kn = kidx(k + PF);
#pragma unroll
        for (int c = 0; c < 5; ++c) bfr[k % PF][c] = BLD(bp + kn * 5120 + c * 1024);
#pragma unroll
        for (int i = 0; i < 4; ++i) af[k % PF][i] = BLD(ap + i * 55296 + kn * 1024);
      }
      __builtin_amdgcn_sched_barrier(0);
    }
    STAMP(1, 4);
    if (w == 7) lds_wait(flg + 2, 4u);
    unsigned base = (w == 7 ? 0 : L2_G + (w - 4) * 20480) + lo;
#pragma unroll
    for (int i = 0; i < 4; ++i)
#pragma unroll
      for (int c = 0; c < 5; ++c) *(LDSP(f32x4))(base + (i * 5 + c) * 1024) = acc[i][c];
    WAITLGKM0();
    BAR();
  }
}

extern "C" void kernel_launch(void* const* d_in, const int* in_sizes, int n_in, void* d_out, int out_size, void* d_ws, size_t ws_size,
                              hipStream_t stream) {
  (void)in_sizes; (void)n_in; (void)out_size; (void)ws_size;
  hipFuncSetAttribute((const void*)k1, hipFuncAttributeMaxDynamicSharedMemorySize, K1_LDS);
  hipFuncSetAttribute((const void*)k2, hipFuncAttributeMaxDynamicSharedMemorySize, K2_LDS);
  k1<<<256, 512, K1_LDS, stream>>>((const char*)d_in[0], (const char*)d_in[1], (const char*)d_in[2], (const char*)d_in[3],
                                   (const char*)d_in[5], (const char*)d_in[6], (char*)d_ws);
  k2<<<256, 512, K2_LDS, stream>>>((const char*)d_in[0], (const char*)d_in[2], (char*)d_ws, (char*)d_out);
}
```

```cpp
#include <hip/hip_runtime.h>
#include <stdint.h>

typedef __attribute__((ext_vector_type(8))) __bf16 bf16x8;
typedef __attribute__((ext_vector_type(2))) __bf16 bf16x2;
typedef __attribute__((ext_vector_type(4))) short s16x4;
typedef __attribute__((ext_vector_type(4))) float f32x4;
typedef __attribute__((ext_vector_type(2))) float f32x2;
typedef __attribute__((ext_vector_type(4))) unsigned u32x4;
typedef __attribute__((ext_vector_type(2))) unsigned u32x2;
#define LDSP(T) __attribute__((address_space(3))) T*
#define GLBP(T) __attribute__((address_space(1))) T*

constexpr size_t WS_HF  = 0;
constexpr size_t WS_OF  = WS_HF + 128ull * 54 * 1024;
constexpr size_t WS_OX  = WS_OF + 32ull * 128 * 512;
constexpr size_t WS_BF  = WS_OF + 128ull * 20 * 1024;
constexpr size_t WS_QF  = WS_BF + 8ull * 74 * 5 * 1024;
constexpr size_t WS_QX  = WS_QF + 32ull * 64 * 1024;
constexpr size_t WS_KVP = WS_QX + 8ull * 64 * 1024;
constexpr size_t WS_WF  = WS_KVP + 32ull * 16 * 3360;
constexpr size_t WS_CNT = WS_WF + 32ull * 2048;

#define STAMP(K, I)
constexpr int STG = 49152;
constexpr int L_WPHI = 3 * STG;
constexpr int L_CS = L_WPHI + 7168;
constexpr int L_ST = L_CS + 1024;
constexpr int K1_LDS = L_ST + 1024;
constexpr int RS = 144;
constexpr int KVI = 128 * RS;

__device__ __forceinline__ unsigned pk2(float a, float b) {
  f32x2 v = {a, b};
  bf16x2 r = __builtin_convertvector(v, bf16x2);
  return __builtin_bit_cast(unsigned, r);
}
__device__ __forceinline__ f32x4 mfma32(u32x4 a, u32x4 b, f32x4 c) {
  return __builtin_amdgcn_mfma_f32_16x16x32_bf16(__builtin_bit_cast(bf16x8, a), __builtin_bit_cast(bf16x8, b), c, 0, 0, 0);
}
__device__ __forceinline__ f32x4 mfma16(u32x2 a, u32x2 b, f32x4 c) {
  return __builtin_amdgcn_mfma_f32_16x16x16bf16_1k(__builtin_bit_cast(s16x4, a), __builtin_bit_cast(s16x4, b), c, 0, 0, 0);
}
__device__ __forceinline__ u32x2 trd(unsigned addr) {
  s16x4 v = __builtin_amdgcn_ds_read_tr16_b64_v4i16((LDSP(s16x4))addr);
  return __builtin_bit_cast(u32x2, v);
}
__device__ __forceinline__ u32x4 trd2(unsigned a0, unsigned a1) {
  u32x2 lo = trd(a0), hi = trd(a1);
  u32x4 r = {lo.x, lo.y, hi.x, hi.y};
  return r;
}
__device__ __forceinline__ u32x4 lds128(unsigned addr) { return *(LDSP(u32x4))addr; }
__device__ __forceinline__ void glds16(const char* g, unsigned ldsbase) {
  __builtin_amdgcn_global_load_lds((GLBP(const void))g, (LDSP(void))ldsbase, 16, 0, 0);
}
__device__ __forceinline__ float asf(unsigned u) { return __builtin_bit_cast(float, u); }
__device__ __forceinline__ unsigned asu(float f) { return __builtin_bit_cast(unsigned, f); }
__device__ __forceinline__ float gsum(float a) {
  auto r = __builtin_amdgcn_permlane16_swap(asu(a), asu(a), false, false);
  float m = asf(r[0]) + asf(r[1]);
  auto r2 = __builtin_amdgcn_permlane32_swap(asu(m), asu(m), false, false);
  return asf(r2[0]) + asf(r2[1]);
}
__device__ __forceinline__ float dot2(unsigned a, unsigned b, float c) {
  asm volatile("v_dot2c_f32_bf16 %0, %1, %2" : "+v"(c) : "v"(a), "v"(b));
  return c;
}
__device__ __forceinline__ float silu_f(float v) {
  float e = __builtin_amdgcn_exp2f(-1.4426950408889634f * v);
  return v * __builtin_amdgcn_rcpf(1.0f + e);
}
#define RSRC(ws) __builtin_amdgcn_make_buffer_rsrc(ws, 0, (int)WS_CNT, 0x00020000)
#define ST16(OFF, V) __builtin_amdgcn_raw_buffer_store_b128(__builtin_bit_cast(u32x4, V), rs, (int)(OFF), 0, 16)
#define LD16(OFF) __builtin_bit_cast(u32x4, __builtin_amdgcn_raw_buffer_load_b128(rs, (int)(OFF), 0, 16))
extern "C" __device__ __attribute__((const)) unsigned long long launch_seq() __asm("llvm.amdgcn.dispatch.id");
__device__ __forceinline__ unsigned long long tag_hi() {
  const unsigned long long pk = (unsigned long long)__builtin_amdgcn_dispatch_ptr();
  return (((launch_seq() + 0x51ull) * 0x9E3779B97F4A7C15ull ^ 0xA5C3D2E1F0B49687ull) + pk * 0xD6E8FEB86659FD93ull) & ~0xffffull;
}
__device__ __forceinline__ unsigned tag_lo() { return (unsigned)(__builtin_amdgcn_s_memrealtime() >> 20) & 0xffffu; }
__device__ __forceinline__ void flag_set(char* ws, int i) {
  __hip_atomic_store((unsigned long long*)(ws + WS_CNT) + i, tag_hi() | tag_lo(), __ATOMIC_RELAXED, __HIP_MEMORY_SCOPE_AGENT);
}
__device__ __forceinline__ bool flag_ok(const char* ws, int i, unsigned long long hi) {
  unsigned long long v = __hip_atomic_load((const unsigned long long*)(ws + WS_CNT) + i, __ATOMIC_RELAXED, __HIP_MEMORY_SCOPE_AGENT);
  return (v & ~0xffffull) == hi && ((tag_lo() - (unsigned)v) & 0xffffu) <= 1u;
}
#define WAITVM_(n) asm volatile("s_waitcnt vmcnt(" #n ")" ::: "memory")
#define WAITVM(n) WAITVM_(n)
#define WAITLGKM0() asm volatile("s_waitcnt lgkmcnt(0)" ::: "memory")
#define BAR() __builtin_amdgcn_s_barrier()

__device__ __forceinline__ void hedge(f32x4 p, float xe, float sc, u32x4& mainv, unsigned& extv) {
  float mx = fmaxf(fmaxf(fmaxf(p[0], p[1]), p[2]), fmaxf(p[3], xe));
  float mn = fminf(fminf(fminf(p[0], p[1]), p[2]), fminf(p[3], xe));
  auto r = __builtin_amdgcn_permlane16_swap(asu(mx), asu(-mn), false, false);
  float m = fmaxf(asf(r[0]), asf(r[1]));
  r = __builtin_amdgcn_permlane32_swap(asu(m), asu(m), false, false);
  m = fmaxf(asf(r[0]), asf(r[1])) * -sc;
  r = __builtin_amdgcn_permlane16_swap(asu(m), asu(m), false, false);
  const f32x2 bb = {asf(r[0]), asf(r[1])}, ss = {sc, -sc};
  f32x2 e[5];
#pragma unroll
  for (int i = 0; i < 5; ++i) {
    float v = i < 4 ? p[i & 3] : xe;
    f32x2 vv = {v, v};
    f32x2 t = __builtin_elementwise_fma(vv, ss, bb);
    e[i][0] = __builtin_amdgcn_exp2f(t[0]);
    e[i][1] = __builtin_amdgcn_exp2f(t[1]);
  }
  f32x2 s = ((e[0] + e[1]) + (e[2] + e[3])) + e[4];
  r = __builtin_amdgcn_permlane16_swap(asu(s[0]), asu(s[1]), false, false);
  m = asf(r[0]) + asf(r[1]);
  r = __builtin_amdgcn_permlane32_swap(asu(m), asu(m), false, false);
  m = __builtin_amdgcn_rcpf(asf(r[0]) + asf(r[1]));
  r = __builtin_amdgcn_permlane16_swap(asu(m), asu(m), false, false);
  const f32x2 rr = {asf(r[0]), asf(r[1])};
#pragma unroll
  for (int i = 0; i < 4; ++i) { f32x2 t = e[i] * rr; mainv[i] = pk2(t[0], t[1]); }
  f32x2 t = e[4] * rr;
  extv = pk2(t[0], t[1]);
}
__device__ __forceinline__ unsigned silu2(float u0, float u1, float c1, float irs) {
  f32x2 u = {u0, u1}, c = {c1, c1}, ir = {irs, irs};
  f32x2 t = u * c;
  f32x2 e = {__builtin_amdgcn_exp2f(t[0]), __builtin_amdgcn_exp2f(t[1])};
  f32x2 d = __builtin_elementwise_fma(e, ir, ir);
  f32x2 rc = {__builtin_amdgcn_rcpf(d[0]), __builtin_amdgcn_rcpf(d[1])};
  f32x2 h = u * rc;
  return pk2(h[0], h[1]);
}

template <int TYPE>
__device__ __forceinline__ void k1_body(const char* __restrict__ x, const char* __restrict__ wqkv, const char* __restrict__ wphi,
                                        const char* __restrict__ wo, const char* __restrict__ w1, const char* __restrict__ w2,
                                        char* __restrict__ ws, int rb, int jj, int bid) {
  constexpr int NT = TYPE == 2 ? 8 : 7;
  const int tid = threadIdx.x, l = tid & 63;
  const int w = __builtin_amdgcn_readfirstlane(tid >> 6);
  const int tg = w & 3, nh = w >> 2;
  const int g = l >> 4, lc = l & 15, q = (l >> 2) & 3, p = l & 3;
  STAMP(0, 0);
  const __amdgpu_buffer_rsrc_t rs = RSRC(ws);
  const char* xp;
  {
    int row = 8 * w + (l >> 3);
    xp = x + (size_t)(rb * 128 + row) * 1280 + (((l & 7) ^ (l >> 3)) << 4);
  }
  auto wsrc = [&](int row, int sl, int& ld) -> const char* {
    const char* wp;
    if (TYPE == 0) {
      if (sl >= 24) { wp = w1 + jj * 128 + (sl - 24) * 16; ld = 3456; }
      else { int s2 = sl < 11 ? (sl > 9 ? 9 : sl) : (sl > 20 ? 9 : sl - 11);
             wp = wqkv + (sl < 11 ? 1280 : 2560) + jj * 160 + s2 * 16; ld = 3840; }
    } else if (TYPE == 1) {
      if (sl >= 24) { wp = w1 + (8 + jj) * 128 + (sl - 24) * 16; ld = 3456; }
      else { int s2 = sl > 19 ? 19 : sl; wp = wqkv + jj * 320 + s2 * 16; ld = 3840; }
    } else {
      int L = 12 + 4 * jj + (sl >> 3); L = L > 26 ? 26 : L;
      wp = w1 + L * 128 + (sl & 7) * 16; ld = 3456;
    }
    return wp + (size_t)row * ld;
  };
  int ld;
  const char* wp;
  {
    int row = 2 * w + (l >> 5);
    wp = wsrc(row, (l & 31) ^ (((row & 3) << 1) | (((row >> 3) & 1) << 3)), ld);
  }
  unsigned tch = 0;
  auto touch = [&]() {
    const int st = w < 7 ? 3 + w : 9;
    const int c8 = l & 7;
    const int sl = TYPE == 0 ? (c8 < 3 ? (c8 * 9 + 1) / 2 : (c8 < 6 ? 11 + ((c8 - 3) * 9 + 1) / 2 : 24 + (c8 - 6) * 4)) : (c8 * 4 + (c8 == 5 ? -1 : 0));
    int ld2;
    const char* a = wsrc(st * 64 + (rb & 7) * 8 + (l >> 3), sl, ld2);
    const char* b = x + (size_t)(rb * 128 + ((bid >> 3) & 3) * 32 + (l & 31)) * 1280 + st * 128;
    asm volatile("global_load_dword %0, %1, off\n\tglobal_load_dword %0, %2, off" : "+v"(tch) : "v"(a), "v"(b));
  };
  auto issue = [&](int s, int buf) {
    unsigned b = buf * STG;
    const char* xs = xp + s * 128;
    glds16(xs, b + w * 1024);
    glds16(xs + 64 * 1280, b + 8192 + w * 1024);
    const char* ws_ = wp + (size_t)(s * 64) * ld;
#pragma unroll
    for (int m = 0; m < 4; ++m) glds16(ws_ + (size_t)(16 * m) * ld, b + 16384 + (w + 8 * m) * 1024);
  };
  auto issue1 = [&](int s, int buf, int i) {
    unsigned b = buf * STG;
    if (i < 2) glds16(xp + s * 128 + i * 64 * 1280, b + i * 8192 + w * 1024);
    else glds16(wp + (size_t)(s * 64 + 16 * (i - 2)) * ld, b + 16384 + (w + 8 * (i - 2)) * 1024);
  };
  issue(0, 0); issue(1, 1); issue(2, 2);
  unsigned ta[NT], ca[2];
  {
    unsigned rowb = 16384 + (8 * g + q) * 512, sw = (q << 2) | ((g & 1) << 4);
    auto grp = [&](int t) -> int {
      if (TYPE == 0) {
        if (t < 2) return 5 * (2 * nh + t) + p;
        if (t == 2) return (p >> 1) * 22 + 10 * nh + 5 * (p & 1) + 4;
        if (t < 5) return 22 + 5 * (2 * nh + t - 3) + p;
        return 48 + 4 * (2 * nh + t - 5) + p;
      } else if (TYPE == 1) {
        if (t < 4) return 5 * (4 * nh + t) + p;
        if (t == 4) return 20 * nh + 5 * p + 4;
        return 48 + 4 * (2 * nh + t - 5) + p;
      } else return 32 * nh + 4 * t + p;
    };
#pragma unroll
    for (int t = 0; t < NT; ++t) ta[t] = rowb + ((grp(t) ^ sw) << 3);
#pragma unroll
    for (int c = 0; c < 2; ++c) {
      int t = 2 * tg + c; t = t >= NT ? NT - 1 : t;
      ca[c] = rowb + ((grp(t) ^ sw) << 3);
    }
  }
  unsigned xa[2][2];
#pragma unroll
  for (int tt = 0; tt < 2; ++tt)
#pragma unroll
    for (int k2 = 0; k2 < 2; ++k2) {
      int row = 32 * tg + 16 * (tt ^ nh) + lc;
      xa[tt][k2] = row * 128 + (((4 * k2 + g) ^ (row & 7)) << 4);
    }
  f32x4 acc[NT][2];
#pragma unroll
  for (int t = 0; t < NT; ++t) { acc[t][0] = (f32x4)(0.f); acc[t][1] = (f32x4)(0.f); }
  unsigned one2 = 0x3f803f80u;
  asm volatile("" : "+v"(one2));
  const int su0 = (rb * 4 + jj) * 6;
  auto side_issue = [&](int u, unsigned lb) {
    int ks = u / 5, cb = u - ks * 5;
    int v = 4 * w + (l >> 4);
    const char* src;
    if (ks < 54) src = w2 + (size_t)(32 * ks + v) * 1280;
    else {
      int k2 = ks - 54, hg = k2 / 5, kk = k2 - hg * 5, row;
      if (kk < 4) row = 20 * (8 * hg + 2 * kk + (v >> 4)) + (v & 15);
      else row = 20 * (8 * hg + 2 * ((v & 15) >> 2) + (v >> 4)) + 16 + (v & 3);
      src = wo + (size_t)row * 1280;
    }
    glds16(src + cb * 256 + (((l & 15) ^ ((v & 7) << 1)) << 4), lb + w * 1024);
  };

  constexpr int NF = NT + 2;
  unsigned fa[NF];
#pragma unroll
  for (int t = 0; t < NT; ++t) fa[t] = ta[t];
  fa[NT] = ca[0]; fa[NT + 1] = ca[1];
  u32x2 wl[2][NF], wh[2][NF];
  u32x4 xf[2][2];
#define RD(S, K2, I)                                                                                              \
  {                                                                                                               \
    if ((I) < 2) asm volatile("ds_read_b128 %0, %1" : "=v"(xf[S][(I) & 1]) : "v"(xa[(I) & 1][K2]));               \
    else if ((I) < 2 + 2 * NF) {                                                                                  \
      if (((I) & 1) == 0) asm volatile("ds_read_b64_tr_b16 %0, %1 offset:%2" : "=v"(wl[S][((I) - 2) >> 1 < NF ? ((I) - 2) >> 1 : 0]) : "v"(fa[((I) - 2) >> 1 < NF ? ((I) - 2) >> 1 : 0]), "i"((K2) * 16384)); \
      else asm volatile("ds_read_b64_tr_b16 %0, %1 offset:%2" : "=v"(wh[S][((I) - 2) >> 1 < NF ? ((I) - 2) >> 1 : 0]) : "v"(fa[((I) - 2) >> 1 < NF ? ((I) - 2) >> 1 : 0]), "i"((K2) * 16384 + 2048)); \
    }                                                                                                             \
  }
#define TIE(S)                                                                                                    \
  {                                                                                                               \
    asm volatile("" : "+v"(xf[S][0]), "+v"(xf[S][1]));                                                            \
    _Pragma("unroll") for (int t = 0; t < NF; ++t) asm volatile("" : "+v"(wl[S][t]), "+v"(wh[S][t]));             \
  }
#define NDBL 5
#define PRE1 0
  f32x4 mcs[2] = {(f32x4)(0.f), (f32x4)(0.f)}, mrs = (f32x4)(0.f), mrq = (f32x4)(0.f);
  const u32x4 ones = {one2, one2, one2, one2};
#define WFR(S, T) ((u32x4){wl[S][T][0], wl[S][T][1], wh[S][T][0], wh[S][T][1]})
#define CUM(I) ((I) + ((I) < NDBL ? (I) : NDBL) + ((I) > 3) + ((I) > 6) + ((I) > 9) + ((I) > 12))
#define PW(S, T) asm volatile("s_waitcnt lgkmcnt(12)" : "+v"(wl[S][T]), "+v"(wh[S][T]))
#define SLOT(S, K2N, DORD, I)                                                                                     \
  {                                                                                                               \
    if (DORD) { RD((S) ^ 1, K2N, CUM(I)); if ((I) < NDBL) RD((S) ^ 1, K2N, CUM(I) + 1); }                         \
    if ((I) == 3 || (I) == 6 || (I) == 9 || (I) == 12) {                                                          \
      __builtin_amdgcn_sched_barrier(0);                                                                          \
      if ((I) == 3) mrs = mfma32(ones, xf[S][0], mrs);                                                            \
      if ((I) == 6) mrq = mfma32(xf[S][0], xf[S][0], mrq);                                                        \
      if ((I) == 9) { if ((S) == 0) PW(S, NT); mcs[0] = mfma32(WFR(S, NT), ones, mcs[0]); }                       \
      if ((I) == 12) { if ((S) == 0) PW(S, NT + 1); mcs[1] = mfma32(WFR(S, NT + 1), ones, mcs[1]); }              \
      if (DORD) RD((S) ^ 1, K2N, CUM((I) + 1) - 1);                                                               \
    }                                                                                                             \
  }
#define KSTEP(S, K2N, DORD, GL)                                                                                   \
  {                                                                                                               \
    _Pragma("unroll") for (int t = 0; t < NT; ++t) {                                                              \
      if ((S) == 0) PW(S, t);                                                                                     \
      u32x4 wf = {wl[S][t][0], wl[S][t][1], wh[S][t][0], wh[S][t][1]};                                            \
      acc[t][0] = mfma32(wf, xf[S][0], acc[t][0]);                                                                \
      SLOT(S, K2N, DORD, 2 * t);                                                                                  \
      __builtin_amdgcn_sched_barrier(0);                                                                          \
      acc[t][1] = mfma32(wf, xf[S][1], acc[t][1]);                                                                \
      SLOT(S, K2N, DORD, 2 * t + 1);                                                                              \
      if ((GL) >= 0 && t >= 1 && t < 7) issue1(GL, (GL) % 3, t - 1);                                              \
      if ((GL) == -2 && TYPE == 1 && t >= 1 && t < 7) side_issue(su0 + t - 1 < 370 ? su0 + t - 1 : 369, STG + (t - 1) * 8192); \
      if ((GL) == -2 && TYPE == 0 && t == 1) { int c = tid < 200 ? tid : 199; if (w < 4) glds16(wphi + (size_t)jj * 3200 + c * 16, L_WPHI + w * 1024); } \
      __builtin_amdgcn_sched_barrier(0);                                                                          \
    }                                                                                                             \
  }
  if (PRE1 == 1) { touch(); WAITVM(14); } else WAITVM(12);
  BAR();
  STAMP(0, 1);
  if (PRE1 == 2) touch();
#pragma unroll
  for (int i = 0; i < 2 + 2 * NF; ++i) RD(0, 0, i);
#pragma unroll
  for (int s = 0; s < 10; ++s) {
    asm volatile("s_waitcnt lgkmcnt(12)" : "+v"(xf[0][0]), "+v"(xf[0][1]));
    __builtin_amdgcn_sched_barrier(0);
    KSTEP(0, 1, true, -1);
    WAITLGKM0();
    TIE(1);
    if (s < 9) {
      if (PRE1 && s < 2) WAITVM(8); else if (s < 8 || TYPE == 1) WAITVM(6); else if (TYPE == 0 && w < 4) WAITVM(1); else WAITVM(0);
      if (s == 2) asm volatile("" ::"v"(tch));
      BAR();
      const int d = (s % 3 == 2) ? -2 * STG : STG;
#pragma unroll
      for (int t = 0; t < NF; ++t) fa[t] += d;
      xa[0][0] += d; xa[0][1] += d; xa[1][0] += d; xa[1][1] += d;
    }
    __builtin_amdgcn_sched_barrier(0);
    KSTEP(1, 0, s < 9, (s <= 6 ? s + 3 : (s == 7 ? -2 : -1)));
  }
  STAMP(0, 2);
  {
    if (lc < 2) *(LDSP(f32x4))(L_CS + nh * 512 + (2 * tg + lc) * 64 + g * 16) = lc ? mcs[1] : mcs[0];
    float sm = mrs[0], sq = p == 0 ? mrq[0] : (p == 1 ? mrq[1] : (p == 2 ? mrq[2] : mrq[3]));
    float mean_ = sm * (1.0f / 640.0f);
    float var = fmaxf(sq * (1.0f / 640.0f) - mean_ * mean_, 0.f) + 1e-5f;
    f32x2 st = {mean_, __builtin_amdgcn_rsqf(var)};
    if (g == q) *(LDSP(f32x2))(L_ST + (32 * tg + 16 * nh + lc) * 8) = st;
  }
  WAITVM(0);
  WAITLGKM0();
  BAR();
  float mean[2], rstd[2];
#pragma unroll
  for (int tt = 0; tt < 2; ++tt) {
    f32x2 st = *(LDSP(f32x2))(L_ST + (32 * tg + 16 * (tt ^ nh) + lc) * 8);
    mean[tt] = st[0]; rstd[tt] = st[1];
  }
#pragma unroll
  for (int t = 0; t < NT; ++t) {
    f32x4 c4 = *(LDSP(f32x4))(L_CS + nh * 512 + t * 64 + g * 16);
#pragma unroll
    for (int tt = 0; tt < 2; ++tt) {
      f32x4 mm = {-mean[tt], -mean[tt], -mean[tt], -mean[tt]};
      acc[t][tt] = __builtin_elementwise_fma(mm, c4, acc[t][tt]);
    }
  }
  STAMP(0, 3);
  if (TYPE == 1) {
    unsigned a = STG + (4 * g + q) * 256 + (((2 * w + (p >> 1)) ^ (((4 * g + q) & 7) << 1)) << 4) + (p & 1) * 8;
#pragma unroll
    for (int i = 0; i < 6; ++i) {
      if (su0 + i < 370) {
        int u = su0 + i, ks = u / 5, cb = u - ks * 5, ct = cb * 8 + w, nb = ct / 5, ci = ct - nb * 5;
        u32x4 f = trd2(a + i * 8192, a + i * 8192 + 4096);
        ST16(WS_BF + ((nb * 74 + ks) * 5 + ci) * 1024 + l * 16, f);
      }
    }
  }
  {
    constexpr int T0 = TYPE == 2 ? 0 : 5;
#pragma unroll
    for (int t = T0; t < NT; t += 2) {
      int ks;
      if (TYPE == 0) ks = 2 * jj + nh;
      else if (TYPE == 1) ks = 2 * (8 + jj) + nh;
      else ks = 2 * (12 + 4 * jj) + 4 * nh + (t >> 1);
      if (ks < 54) {
#pragma unroll
        for (int tt = 0; tt < 2; ++tt) {
          const float c1 = rstd[tt] * -1.4426950408889634f, irs = __builtin_amdgcn_rcpf(rstd[tt]);
          u32x4 o = {silu2(acc[t][tt][0], acc[t][tt][1], c1, irs), silu2(acc[t][tt][2], acc[t][tt][3], c1, irs),
                     silu2(acc[t + 1][tt][0], acc[t + 1][tt][1], c1, irs), silu2(acc[t + 1][tt][2], acc[t + 1][tt][3], c1, irs)};
          int gtt = rb * 8 + 2 * tg + (tt ^ nh);
          ST16(WS_HF + (gtt * 54 + ks) * 1024 + l * 16, o);
        }
      }
    }
  }
  STAMP(0, 4);
  if (TYPE == 2) return;
  if (TYPE == 1) {
    const int tp = rb * 4 + tg;
#pragma unroll
    for (int t = 0; t < 5; ++t) {
      f32x4 v0 = acc[t][0] * rstd[0], v1 = acc[t][1] * rstd[1];
      u32x2 a = {pk2(v0[0], v0[1]), pk2(v0[2], v0[3])}, b = {pk2(v1[0], v1[1]), pk2(v1[2], v1[3])};
      u32x4 o = nh ? (u32x4){b[0], b[1], a[0], a[1]} : (u32x4){a[0], a[1], b[0], b[1]};
      int dst = t < 4 ? (int)WS_QF + ((jj * 8 + nh * 4 + t) * 64 + tp) * 1024 : (int)WS_QX + ((jj * 2 + nh) * 64 + tp) * 1024;
      ST16(dst + l * 16, o);
    }
    {
      const u32x4 sen = {0xffffffffu, 0xffffffffu, 0xffffffffu, 0xffffffffu};
      int dst = (int)WS_OF + (rb * 4 + jj) * 40960 + tid * 16;
#pragma unroll
      for (int i = 0; i < 5; ++i) ST16(dst + i * 8192, sen);
    }
    STAMP(0, 5);
    return;
  }
  u32x2 xb[2];
#pragma unroll
  for (int tt = 0; tt < 2; ++tt) { xb[tt][0] = pk2(acc[2][tt][0], acc[2][tt][1]); xb[tt][1] = pk2(acc[2][tt][2], acc[2][tt][3]); }
#pragma unroll
  for (int hh = 0; hh < 2; ++hh) {
    const int hl = 2 * nh + hh;
    unsigned wb = L_WPHI + hl * 800;
    u32x4 a0 = trd2(wb + (4 * g + q) * 40 + p * 8, wb + (16 + q) * 40 + p * 8);
    u32x4 a1;
    {
      unsigned b1 = wb + 4 * g * 40 + 32 + q * 2, b2 = wb + 640 + 32 + q * 2;
      a1[0] = (unsigned)*(LDSP(unsigned short))(b1) | ((unsigned)*(LDSP(unsigned short))(b1 + 40) << 16);
      a1[1] = (unsigned)*(LDSP(unsigned short))(b1 + 80) | ((unsigned)*(LDSP(unsigned short))(b1 + 120) << 16);
      a1[2] = (unsigned)*(LDSP(unsigned short))(b2) | ((unsigned)*(LDSP(unsigned short))(b2 + 40) << 16);
      a1[3] = (unsigned)*(LDSP(unsigned short))(b2 + 80) | ((unsigned)*(LDSP(unsigned short))(b2 + 120) << 16);
    }
    if (rb == 0 && tg == 0) {
      int wf = (int)WS_WF + (jj * 4 + hl) * 2048 + l * 16;
      ST16(wf, a0); ST16(wf + 1024, a1);
    }
    if (g != hh) { a0[2] = 0; a0[3] = 0; a1[2] = 0; a1[3] = 0; }
#pragma unroll
    for (int tt = 0; tt < 2; ++tt) {
      u32x4 kb = {pk2(acc[hh][tt][0], acc[hh][tt][1]), pk2(acc[hh][tt][2], acc[hh][tt][3]), xb[tt][0], xb[tt][1]};
      f32x4 pm = mfma32(a0, kb, (f32x4)(0.f));
      f32x4 px = mfma32(a1, kb, (f32x4)(0.f));
      u32x4 mv; unsigned ev;
      hedge(pm, px[0], rstd[tt] * 1.4426950408889634f, mv, ev);
      unsigned ib = (nh ? 0 : 2 * STG) + hh * KVI + (32 * tg + 16 * (tt ^ nh) + lc) * RS;
      *(LDSP(u32x4))(ib + g * 16) = mv;
      *(LDSP(unsigned))(ib + 64 + g * 4) = ev;
      f32x4 v = acc[3 + hh][tt] * rstd[tt];
      u32x2 vv = {pk2(v[0], v[1]), pk2(v[2], v[3])};
      *(LDSP(u32x2))(ib + 80 + g * 8) = vv;
    }
  }
  STAMP(0, 5);
  {
#pragma unroll
    for (int tt = 0; tt < 2; ++tt) {
      unsigned ib = (nh ? 0 : 2 * STG) + (g & 1) * KVI + (32 * tg + 16 * (tt ^ nh) + lc) * RS + (g >= 2 ? 112 : 120);
      f32x4 v = acc[2][tt] * rstd[tt];
      u32x2 vv = {pk2(v[0], v[1]), pk2(v[2], v[3])};
      if (g < 2) { vv[0] = 0x00003f80u; vv[1] = 0u; }
      *(LDSP(u32x2))(ib) = vv;
    }
    WAITVM(0);
    WAITLGKM0();
    BAR();
    if (tid < 2) flag_set(ws, 2048 + tid * 256 + rb * 16 + (jj >> 1) * 4 + (jj & 1));
    const int hl = w >> 1, dt = w & 1;
    unsigned ib = (hl < 2 ? 2 * STG : 0) + (hl & 1) * KVI + (4 * g + q) * RS + p * 8;
    f32x4 kv0 = (f32x4)(0.f), kv1 = (f32x4)(0.f), kv2 = (f32x4)(0.f);
#pragma unroll
    for (int k = 0; k < 4; ++k) {
      unsigned a = ib + k * 32 * RS;
      u32x4 bv = trd2(a + 80 + dt * 32, a + 80 + dt * 32 + 16 * RS);
      kv0 = mfma32(trd2(a, a + 16 * RS), bv, kv0);
      kv1 = mfma32(trd2(a + 32, a + 32 + 16 * RS), bv, kv1);
      kv2 = mfma32(trd2(a + 64, a + 64 + 16 * RS), bv, kv2);
    }
    if (dt == 0 || lc < 5) {
      int col = 16 * dt + lc;
      int o = (int)WS_KVP + ((jj * 4 + hl) * 16 + rb) * 3360 + col * 160 + g * 16;
      ST16(o, kv0);
      ST16(o + 64, kv1);
      if (g < 2) ST16(o + 128, kv2);
    }
    STAMP(0, 6);
  }
}

constexpr int L2_G = 102400;
constexpr int L2_S = 97280;
constexpr int K2_LDS = 163840;
#define PF 3
#define HP 2
#define SLP2 4
#define WOD 58
#define WB 90
#define PRE 0
#define PRE0 6
#define SLP3 2
#define GB 0
#define AUX 16
#define ROT 0
#define ROTX 0
#define MAP 1
#define SLP0 8
#define SLP1 4
__device__ __forceinline__ void lds_wait(LDSP(unsigned) f, unsigned v) {
  for (int sp = 0; sp < (1 << 22); ++sp) {
    if (__hip_atomic_load(f, __ATOMIC_RELAXED, __HIP_MEMORY_SCOPE_WORKGROUP) >= v) break;
    __builtin_amdgcn_s_sleep(1);
  }
  asm volatile("" ::: "memory");
}
__global__ __launch_bounds__(512, 2) void kf(const char* __restrict__ x, const char* __restrict__ wqkv, const char* __restrict__ wphi,
                                             const char* __restrict__ wo, const char* __restrict__ w1, const char* __restrict__ w2,
                                             char* __restrict__ ws, char* __restrict__ out) {
  const int bid = blockIdx.x, xcd = bid & 7, idx = bid >> 3;
  {
    const int rb = (xcd >> 2) * 8 + (idx >> 2), j = xcd & 3, ty = idx & 3;
    if (ty < 2) k1_body<0>(x, wqkv, wphi, wo, w1, w2, ws, rb, 2 * j + ty, bid);
    else if (ty == 2) k1_body<1>(x, wqkv, wphi, wo, w1, w2, ws, rb, j, bid);
    else k1_body<2>(x, wqkv, wphi, wo, w1, w2, ws, rb, j, bid);
    __builtin_amdgcn_s_waitcnt(0);
    WAITVM(0);
    BAR();
    const int t = threadIdx.x;
    if (ty < 2) { if (t < 4) flag_set(ws, 2560 + t * 128 + (2 * j + ty) * 16 + rb); }
    else if (t < 2) flag_set(ws, 2048 + t * 256 + rb * 16 + j * 4 + ty);
    else if (ty == 2 && t < 34) flag_set(ws, (t - 2) * 64 + rb * 4 + j);
  }
  const __amdgpu_buffer_rsrc_t rs = RSRC(ws);
  const int mb = 8 * (xcd >> 1) + (idx >> 2), nb = 4 * (xcd & 1) + ((idx + (idx >> 2)) & 3);
  const int tgp = idx >> 2;
  const int tid = threadIdx.x, l = tid & 63;
  const int w = __builtin_amdgcn_readfirstlane(tid >> 6);
  const int g = l >> 4, lc = l & 15;
  const unsigned lo = l * 16;
  STAMP(1, 0);
  LDSP(unsigned) flg = (LDSP(unsigned))(L2_S + 2048);
  if (w < 4) {
    __builtin_amdgcn_s_setprio(HP);
    const int head = xcd * 4 + (idx & 3);
    if (tid < 8) flg[tid] = 0u;
    WAITLGKM0();
    BAR();
    lds_wait(flg + 1, 1u);
    u32x4 qf[2], qx[2];
#pragma unroll
    for (int i = 0; i < 2; ++i) {
      int tp = tgp * 8 + w * 2 + i;
      qf[i] = LD16(WS_QF + (head * 64 + tp) * 1024 + lo);
      qx[i] = LD16(WS_QX + ((head >> 2) * 64 + tp) * 1024 + lo);
    }
    if (w == 0) {
      const unsigned long long hi = tag_hi();
      for (int sp = 0; sp < (1 << 18); ++sp) {
        bool ok = flag_ok(ws, 2560 + (idx & 3) * 128 + xcd * 16 + lc, hi);
        if (__builtin_amdgcn_ballot_w64(!ok) == 0ull) break;
        __builtin_amdgcn_s_sleep(4);
      }
      if (l == 0) __hip_atomic_store(flg + 4, 1u, __ATOMIC_RELAXED, __HIP_MEMORY_SCOPE_WORKGROUP);
    } else lds_wait(flg + 4, 1u);
    asm volatile("" ::: "memory");
    STAMP(1, 7);
    u32x4 a0 = LD16(WS_WF + head * 2048 + lo), a1 = LD16(WS_WF + head * 2048 + 1024 + lo);
    f32x4 part[16];
    const int kp = (int)WS_KVP + head * 16 * 3360 + (tid < 210 ? tid : 209) * 16;
#pragma unroll
    for (int i = 0; i < 16; ++i) part[i] = __builtin_bit_cast(f32x4, LD16(kp + i * 3360));
    __builtin_amdgcn_sched_barrier(0);
    if (g != (head & 3)) { a0[2] = 0; a0[3] = 0; a1[2] = 0; a1[3] = 0; }
    u32x4 pq[4]; unsigned px[4];
#pragma unroll
    for (int i = 0; i < 4; ++i) {
      u32x4 kb = {qf[i >> 1][2 * (i & 1)], qf[i >> 1][2 * (i & 1) + 1], qx[i >> 1][2 * (i & 1)], qx[i >> 1][2 * (i & 1) + 1]};
      f32x4 pm = mfma32(a0, kb, (f32x4)(0.f));
      f32x4 pe = mfma32(a1, kb, (f32x4)(0.f));
      hedge(pm, pe[0], 1.4426950408889634f, pq[i], px[i]);
    }
    __builtin_amdgcn_sched_barrier(0);
    f32x4 s = ((part[0] + part[1]) + (part[2] + part[3])) + ((part[4] + part[5]) + (part[6] + part[7])) +
              (((part[8] + part[9]) + (part[10] + part[11])) + ((part[12] + part[13]) + (part[14] + part[15])));
    u32x2 sb = {pk2(s[0], s[1]), pk2(s[2], s[3])};
    if (tid < 210) *(LDSP(u32x2))(L2_S + tid * 8) = sb;
    WAITLGKM0();
    if (l == 0) __hip_atomic_fetch_add(flg, 1u, __ATOMIC_RELAXED, __HIP_MEMORY_SCOPE_WORKGROUP);
    lds_wait(flg, 4u);
    STAMP(1, 1);
    {
      const int c1 = 16 + (lc & 3);
      u32x4 b0 = lds128(L2_S + lc * 80 + g * 16), b1 = lds128(L2_S + c1 * 80 + g * 16), b2 = lds128(L2_S + 1600 + g * 16);
      u32x4 e0 = {*(LDSP(unsigned))(L2_S + lc * 80 + 64 + g * 4), 0u, 0u, 0u}, e1 = {*(LDSP(unsigned))(L2_S + c1 * 80 + 64 + g * 4), 0u, 0u, 0u},
            e2 = {*(LDSP(unsigned))(L2_S + 1600 + 64 + g * 4), 0u, 0u, 0u};
      unsigned long long oe = 0;
#pragma unroll
      for (int i = 0; i < 4; ++i) {
        u32x4 pe = {px[i], 0u, 0u, 0u};
        f32x4 d2 = mfma32(b2, pq[i], (f32x4)(0.f));
        f32x4 d0 = mfma32(b0, pq[i], (f32x4)(0.f));
        f32x4 d1 = mfma32(b1, pq[i], (f32x4)(0.f));
        d2 = mfma32(e2, pe, d2);
        d0 = mfma32(e0, pe, d0);
        d1 = mfma32(e1, pe, d1);
        float inv = __builtin_amdgcn_rcpf(d2[0] + 1e-6f);
        unsigned long long ov = (unsigned long long)pk2(d0[0] * inv, d0[1] * inv) | ((unsigned long long)pk2(d0[2] * inv, d0[3] * inv) << 32);
        unsigned long long ev = (unsigned long long)pk2(d1[0] * inv, d1[1] * inv) | ((unsigned long long)pk2(d1[2] * inv, d1[3] * inv) << 32);
        if (g == i) oe = ev;
        int gtt = tgp * 16 + w * 4 + i;
        __hip_atomic_store((unsigned long long*)(ws + WS_OF + ((size_t)head * 128 + gtt) * 512 + l * 8), ov, __ATOMIC_RELAXED, __HIP_MEMORY_SCOPE_AGENT);
      }
      __hip_atomic_store((unsigned long long*)(ws + WS_OX + ((size_t)((tgp * 16 + w * 4 + g) * 4 + (head >> 3)) * 2 + (head & 1)) * 512 + ((head & 7) >> 1) * 128 + lc * 8),
                         oe, __ATOMIC_RELAXED, __HIP_MEMORY_SCOPE_AGENT);
    }
    STAMP(1, 2);
    __builtin_amdgcn_sched_barrier(0);
    u32x4 bl[5];
#pragma unroll
    for (int c = 0; c < 5; ++c) bl[c] = LD16(WS_BF + (nb * 74 + 73) * 5120 + c * 1024 + lo);
    const size_t orow = (size_t)(mb * 64 + w * 16 + lc) * 1280 + nb * 160 + g * 8;
    u32x2 xv[5];
#pragma unroll
    for (int c = 0; c < 5; ++c) xv[c] = *(const u32x2*)(x + orow + c * 32);
    __builtin_amdgcn_sched_barrier(0);
    unsigned long long om[32], ox[8];
    {
      const char* op = ws + WS_OF + (size_t)(mb * 4 + w) * 512 + l * 8;
      const char* oxp = ws + WS_OX + (size_t)(mb * 4 + w) * 4096 + l * 8;
      unsigned need = 15u;
      __builtin_amdgcn_s_sleep(SLP0);
      for (int sp = 0; sp < (1 << 16); ++sp) {
#pragma unroll
        for (int hg = 0; hg < 4; ++hg)
          if (need & (1u << hg)) {
#pragma unroll
            for (int j = 0; j < 8; ++j)
              om[8 * hg + j] = __hip_atomic_load((const unsigned long long*)(op + (size_t)(8 * hg + j) * 65536), __ATOMIC_RELAXED, __HIP_MEMORY_SCOPE_AGENT);
#pragma unroll
            for (int j = 0; j < 2; ++j)
              ox[2 * hg + j] = __hip_atomic_load((const unsigned long long*)(oxp + (2 * hg + j) * 512), __ATOMIC_RELAXED, __HIP_MEMORY_SCOPE_AGENT);
          }
#pragma unroll
        for (int hg = 0; hg < 4; ++hg)
          if (need & (1u << hg)) {
            unsigned m = max(max((unsigned)ox[2 * hg], (unsigned)(ox[2 * hg] >> 32)), max((unsigned)ox[2 * hg + 1], (unsigned)(ox[2 * hg + 1] >> 32)));
#pragma unroll
            for (int j = 0; j < 8; ++j) m = max(max(m, (unsigned)om[8 * hg + j]), (unsigned)(om[8 * hg + j] >> 32));
            if (__builtin_amdgcn_ballot_w64(m == 0xffffffffu) == 0ull) need &= ~(1u << hg);
          }
        if (need == 0u) break;
        __builtin_amdgcn_s_sleep(SLP1);
      }
    }
    STAMP(1, 3);
    __builtin_amdgcn_sched_barrier(0);
    lds_wait(flg + 3, 4u);
    u32x4 wf[3][5];
#pragma unroll
    for (int k = 0; k < 3; ++k)
#pragma unroll
      for (int c = 0; c < 5; ++c) wf[k][c] = lds128((k * 5 + c) * 1024 + lo);
    STAMP(1, 6);
    f32x4 acc[5];
#pragma unroll
    for (int c = 0; c < 5; ++c) acc[c] = (f32x4)(0.f);
#pragma unroll
    for (int k = 0; k < 20; ++k) {
      const int hg = k / 5, kk = k % 5;
      const unsigned long long f0 = kk < 4 ? om[8 * hg + 2 * (kk & 3)] : ox[2 * hg], f1 = kk < 4 ? om[8 * hg + 2 * (kk & 3) + 1] : ox[2 * hg + 1];
      u32x4 af = {(unsigned)f0, (unsigned)(f0 >> 32), (unsigned)f1, (unsigned)(f1 >> 32)};
#pragma unroll
      for (int c = 0; c < 5; ++c) {
        acc[c] = mfma32(k < 19 ? wf[k % 3][c] : bl[c], af, acc[c]);
        if (k + 3 < 19) wf[k % 3][c] = lds128(((k + 3) * 5 + c) * 1024 + lo);
        __builtin_amdgcn_sched_barrier(0);
      }
    }
    __builtin_amdgcn_s_setprio(0);
    if (l == 0) __hip_atomic_fetch_add(flg + 2, 1u, __ATOMIC_RELAXED, __HIP_MEMORY_SCOPE_WORKGROUP);
    STAMP(1, 4);
    BAR();
#pragma unroll
    for (int c = 0; c < 5; ++c) {
      unsigned a = L2_G + (w * 5 + c) * 1024 + lo;
      f32x4 gv = (*(LDSP(f32x4))(a) + *(LDSP(f32x4))(a + 20480)) + (*(LDSP(f32x4))(a + 40960) + *(LDSP(f32x4))(a - L2_G));
      float o[4];
#pragma unroll
      for (int r = 0; r < 4; ++r) {
        float xf = __builtin_bit_cast(float, (r & 1) ? (xv[c][r >> 1] & 0xffff0000u) : (xv[c][r >> 1] << 16));
        o[r] = __builtin_fmaf(acc[c][r], silu_f(gv[r]), xf);
      }
      u32x2 ov = {pk2(o[0], o[1]), pk2(o[2], o[3])};
      *(u32x2*)(out + orow + c * 32) = ov;
    }
    STAMP(1, 5);
  } else {
    f32x4 acc[4][5];
#pragma unroll
    for (int i = 0; i < 4; ++i)
#pragma unroll
      for (int c = 0; c < 5; ++c) acc[i][c] = (f32x4)(0.f);
    const int k0 = (w - 4) * 14 - (w == 7 ? 1 : 0);
    const int nk = w < 6 ? 14 : 13;
    const int rot = ROT ? (2 * (idx & 7) + 7 * ((idx >> 3) & 1) + 3 * (idx >> 4) + ROTX * xcd) % 13 : 0;
    auto kidx = [&](int k) { int kn = (k < nk ? k : nk - 1) + rot; return kn >= nk ? kn - nk : kn; };
    const int ap = (int)WS_HF + (mb * 4 * 54 + k0) * 1024, bp = (int)WS_BF + (nb * 74 + k0) * 5120;
#define BLD(OFF) __builtin_bit_cast(u32x4, __builtin_amdgcn_raw_buffer_load_b128(rs, lo, (OFF), AUX))
    u32x4 af[PF][4], bfr[PF][5];
    const int f0 = w == 7 ? 71 : (w - 4) * 24;
    const char* wp = ws + WS_BF + (size_t)(nb * 74 + 54) * 5120 + f0 * 1024 + lo;
    BAR();
    if (w == 4) {
      const unsigned long long hi = tag_hi();
      for (int sp = 0; sp < (1 << 18); ++sp) {
        bool ok = flag_ok(ws, idx * 64 + l, hi) & flag_ok(ws, 2048 + (mb & 1) * 256 + (mb >> 1) * 16 + lc, hi);
        if (__builtin_amdgcn_ballot_w64(!ok) == 0ull) break;
        __builtin_amdgcn_s_sleep(4);
      }
      if (l == 0) __hip_atomic_store(flg + 1, 1u, __ATOMIC_RELAXED, __HIP_MEMORY_SCOPE_WORKGROUP);
    } else lds_wait(flg + 1, 1u);
    asm volatile("" ::: "memory");
    STAMP(1, 7);
#pragma unroll
    for (int i0 = 0; i0 < 6; ++i0) {
      const int i = ROT ? (i0 + (idx & 7)) % 6 : i0;
      const char* a = wp + i * 4096;
      asm volatile("s_mov_b32 m0, %1\n\ts_nop 0\n\tglobal_load_lds_dwordx4 %0, off sc1\n\tglobal_load_lds_dwordx4 %0, off offset:1024 sc1\n\t"
                   "global_load_lds_dwordx4 %0, off offset:2048 sc1\n\tglobal_load_lds_dwordx4 %0, off offset:3072 sc1"
                   ::"v"(a), "s"((f0 + 4 * i) * 1024) : "memory");
    }
#pragma unroll
    for (int k = 0; k < PF; ++k) {
      const int kn = kidx(k);
#pragma unroll
      for (int c = 0; c < 5; ++c) bfr[k][c] = BLD(bp + kn * 5120 + c * 1024);
#pragma unroll
      for (int i = 0; i < 4; ++i) af[k][i] = BLD(ap + i * 55296 + kn * 1024);
    }
    __builtin_amdgcn_sched_barrier(0);
#pragma unroll
    for (int k = 0; k < 14; ++k) {
      if (k == 13 && w >= 6) {
#pragma unroll
        for (int i = 0; i < 4; ++i) af[k % PF][i] = (u32x4)(0u);
      }
#pragma unroll
      for (int i = 0; i < 4; ++i)
#pragma unroll
        for (int c = 0; c < 5; ++c) acc[i][c] = mfma32(bfr[k % PF][c], af[k % PF][i], acc[i][c]);
      __builtin_amdgcn_sched_barrier(0);
      if (k == 0 && l == 0) __hip_atomic_fetch_add(flg + 3, 1u, __ATOMIC_RELAXED, __HIP_MEMORY_SCOPE_WORKGROUP);
      if (k + PF < 14) {
        const int kn = kidx(k + PF);
#pragma unroll
        for (int c = 0; c < 5; ++c) bfr[k % PF][c] = BLD(bp + kn * 5120 + c * 1024);
#pragma unroll
        for (int i = 0; i < 4; ++i) af[k % PF][i] = BLD(ap + i * 55296 + kn * 1024);
      }
      __builtin_amdgcn_sched_barrier(0);
    }
    STAMP(1, 4);
    if (w == 7) lds_wait(flg + 2, 4u);
    unsigned base = (w == 7 ? 0 : L2_G + (w - 4) * 20480) + lo;
#pragma unroll
    for (int i = 0; i < 4; ++i)
#pragma unroll
      for (int c = 0; c < 5; ++c) *(LDSP(f32x4))(base + (i * 5 + c) * 1024) = acc[i][c];
    WAITLGKM0();
    BAR();
  }
}

extern "C" void kernel_launch(void* const* d_in, const int* in_sizes, int n_in, void* d_out, int out_size, void* d_ws, size_t ws_size,
                              hipStream_t stream) {
  (void)in_sizes; (void)n_in; (void)out_size; (void)ws_size;
  hipFuncSetAttribute((const void*)kf, hipFuncAttributeMaxDynamicSharedMemorySize, K2_LDS);
  kf<<<256, 512, K2_LDS, stream>>>((const char*)d_in[0], (const char*)d_in[1], (const char*)d_in[2], (const char*)d_in[3],
                                   (const char*)d_in[5], (const char*)d_in[6], (char*)d_ws, (char*)d_out);
}
```

```cpp
#include <hip/hip_runtime.h>
#include <stdint.h>

typedef __attribute__((ext_vector_type(8))) __bf16 bf16x8;
typedef __attribute__((ext_vector_type(2))) __bf16 bf16x2;
typedef __attribute__((ext_vector_type(4))) short s16x4;
typedef __attribute__((ext_vector_type(4))) float f32x4;
typedef __attribute__((ext_vector_type(2))) float f32x2;
typedef __attribute__((ext_vector_type(4))) unsigned u32x4;
typedef __attribute__((ext_vector_type(2))) unsigned u32x2;
#define LDSP(T) __attribute__((address_space(3))) T*
#define GLBP(T) __attribute__((address_space(1))) T*

constexpr size_t WS_HF  = 0;
constexpr size_t WS_OF  = WS_HF + 128ull * 54 * 1024;
constexpr size_t WS_OX  = WS_OF + 32ull * 128 * 512;
constexpr size_t WS_BF  = WS_OF + 128ull * 20 * 1024;
constexpr size_t WS_QF  = WS_BF + 8ull * 74 * 5 * 1024;
constexpr size_t WS_QX  = WS_QF + 32ull * 64 * 1024;
constexpr size_t WS_KVP = WS_QX + 8ull * 64 * 1024;
constexpr size_t WS_WF  = WS_KVP + 32ull * 16 * 3360;
constexpr size_t WS_CNT = WS_WF + 32ull * 2048;

#define STAMP(K, I)
constexpr int STG = 49152;
constexpr int L_WPHI = 3 * STG;
constexpr int L_CS = L_WPHI + 7168;
constexpr int L_ST = L_CS + 1024;
constexpr int K1_LDS = L_ST + 1024;
constexpr int RS = 144;
constexpr int KVI = 128 * RS;

__device__ __forceinline__ unsigned pk2(float a, float b) {
  f32x2 v = {a, b};
  bf16x2 r = __builtin_convertvector(v, bf16x2);
  return __builtin_bit_cast(unsigned, r);
}
__device__ __forceinline__ f32x4 mfma32(u32x4 a, u32x4 b, f32x4 c) {
  return __builtin_amdgcn_mfma_f32_16x16x32_bf16(__builtin_bit_cast(bf16x8, a), __builtin_bit_cast(bf16x8, b), c, 0, 0, 0);
}
__device__ __forceinline__ f32x4 mfma16(u32x2 a, u32x2 b, f32x4 c) {
  return __builtin_amdgcn_mfma_f32_16x16x16bf16_1k(__builtin_bit_cast(s16x4, a), __builtin_bit_cast(s16x4, b), c, 0, 0, 0);
}
__device__ __forceinline__ u32x2 trd(unsigned addr) {
  s16x4 v = __builtin_amdgcn_ds_read_tr16_b64_v4i16((LDSP(s16x4))addr);
  return __builtin_bit_cast(u32x2, v);
}
__device__ __forceinline__ u32x4 trd2(unsigned a0, unsigned a1) {
  u32x2 lo = trd(a0), hi = trd(a1);
  u32x4 r = {lo.x, lo.y, hi.x, hi.y};
  return r;
}
__device__ __forceinline__ u32x4 lds128(unsigned addr) { return *(LDSP(u32x4))addr; }
__device__ __forceinline__ void glds16(const char* g, unsigned ldsbase) {
  __builtin_amdgcn_global_load_lds((GLBP(const void))g, (LDSP(void))ldsbase, 16, 0, 0);
}
__device__ __forceinline__ float asf(unsigned u) { return __builtin_bit_cast(float, u); }
__device__ __forceinline__ unsigned asu(float f) { return __builtin_bit_cast(unsigned, f); }
__device__ __forceinline__ float gsum(float a) {
  auto r = __builtin_amdgcn_permlane16_swap(asu(a), asu(a), false, false);
  float m = asf(r[0]) + asf(r[1]);
  auto r2 = __builtin_amdgcn_permlane32_swap(asu(m), asu(m), false, false);
  return asf(r2[0]) + asf(r2[1]);
}
__device__ __forceinline__ float dot2(unsigned a, unsigned b, float c) {
  asm volatile("v_dot2c_f32_bf16 %0, %1, %2" : "+v"(c) : "v"(a), "v"(b));
  return c;
}
__device__ __forceinline__ float silu_f(float v) {
  float e = __builtin_amdgcn_exp2f(-1.4426950408889634f * v);
  return v * __builtin_amdgcn_rcpf(1.0f + e);
}
#define CODE_PF 57344
#define RSRC(ws) __builtin_amdgcn_make_buffer_rsrc(ws, 0, (int)WS_CNT, 0x00020000)
#define ST16(OFF, V) __builtin_amdgcn_raw_buffer_store_b128(__builtin_bit_cast(u32x4, V), rs, (int)(OFF), 0, 16)
#define LD16(OFF) __builtin_bit_cast(u32x4, __builtin_amdgcn_raw_buffer_load_b128(rs, (int)(OFF), 0, 16))
extern "C" __device__ __attribute__((const)) unsigned long long launch_seq() __asm("llvm.amdgcn.dispatch.id");
__device__ __forceinline__ unsigned long long tag_hi() {
  const unsigned long long pk = (unsigned long long)__builtin_amdgcn_dispatch_ptr();
  return (((launch_seq() + 0x51ull) * 0x9E3779B97F4A7C15ull ^ 0xA5C3D2E1F0B49687ull) + pk * 0xD6E8FEB86659FD93ull) & ~0xffffull;
}
__device__ __forceinline__ unsigned tag_lo() { return (unsigned)(__builtin_amdgcn_s_memrealtime() >> 20) & 0xffffu; }
__device__ __forceinline__ void flag_set(char* ws, int i) {
  __hip_atomic_store((unsigned long long*)(ws + WS_CNT) + i, tag_hi() | tag_lo(), __ATOMIC_RELAXED, __HIP_MEMORY_SCOPE_AGENT);
}
__device__ __forceinline__ bool flag_ok(const char* ws, int i, unsigned long long hi) {
  unsigned long long v = __hip_atomic_load((const unsigned long long*)(ws + WS_CNT) + i, __ATOMIC_RELAXED, __HIP_MEMORY_SCOPE_AGENT);
  return (v & ~0xffffull) == hi && ((tag_lo() - (unsigned)v) & 0xffffu) <= 1u;
}
#define WAITVM_(n) asm volatile("s_waitcnt vmcnt(" #n ")" ::: "memory")
#define WAITVM(n) WAITVM_(n)
#define WAITLGKM0() asm volatile("s_waitcnt lgkmcnt(0)" ::: "memory")
#define BAR() __builtin_amdgcn_s_barrier()

__device__ __forceinline__ void hedge(f32x4 p, float xe, float sc, u32x4& mainv, unsigned& extv) {
  float mx = fmaxf(fmaxf(fmaxf(p[0], p[1]), p[2]), fmaxf(p[3], xe));
  float mn = fminf(fminf(fminf(p[0], p[1]), p[2]), fminf(p[3], xe));
  auto r = __builtin_amdgcn_permlane16_swap(asu(mx), asu(-mn), false, false);
  float m = fmaxf(asf(r[0]), asf(r[1]));
  r = __builtin_amdgcn_permlane32_swap(asu(m), asu(m), false, false);
  m = fmaxf(asf(r[0]), asf(r[1])) * -sc;
  r = __builtin_amdgcn_permlane16_swap(asu(m), asu(m), false, false);
  const f32x2 bb = {asf(r[0]), asf(r[1])}, ss = {sc, -sc};
  f32x2 e[5];
#pragma unroll
  for (int i = 0; i < 5; ++i) {
    float v = i < 4 ? p[i & 3] : xe;
    f32x2 vv = {v, v};
    f32x2 t = __builtin_elementwise_fma(vv, ss, bb);
    e[i][0] = __builtin_amdgcn_exp2f(t[0]);
    e[i][1] = __builtin_amdgcn_exp2f(t[1]);
  }
  f32x2 s = ((e[0] + e[1]) + (e[2] + e[3])) + e[4];
  r = __builtin_amdgcn_permlane16_swap(asu(s[0]), asu(s[1]), false, false);
  m = asf(r[0]) + asf(r[1]);
  r = __builtin_amdgcn_permlane32_swap(asu(m), asu(m), false, false);
  m = __builtin_amdgcn_rcpf(asf(r[0]) + asf(r[1]));
  r = __builtin_amdgcn_permlane16_swap(asu(m), asu(m), false, false);
  const f32x2 rr = {asf(r[0]), asf(r[1])};
#pragma unroll
  for (int i = 0; i < 4; ++i) { f32x2 t = e[i] * rr; mainv[i] = pk2(t[0], t[1]); }
  f32x2 t = e[4] * rr;
  extv = pk2(t[0], t[1]);
}
__device__ __forceinline__ unsigned silu2(float u0, float u1, float c1, float irs) {
  f32x2 u = {u0, u1}, c = {c1, c1}, ir = {irs, irs};
  f32x2 t = u * c;
  f32x2 e = {__builtin_amdgcn_exp2f(t[0]), __builtin_amdgcn_exp2f(t[1])};
  f32x2 d = __builtin_elementwise_fma(e, ir, ir);
  f32x2 rc = {__builtin_amdgcn_rcpf(d[0]), __builtin_amdgcn_rcpf(d[1])};
  f32x2 h = u * rc;
  return pk2(h[0], h[1]);
}

template <int TYPE>
__device__ __forceinline__ void k1_body(const char* __restrict__ x, const char* __restrict__ wqkv, const char* __restrict__ wphi,
                                        const char* __restrict__ wo, const char* __restrict__ w1, const char* __restrict__ w2,
                                        char* __restrict__ ws, int rb, int jj, int bid, unsigned long long pc) {
  constexpr int NT = TYPE == 2 ? 8 : 7;
  const int tid = threadIdx.x, l = tid & 63;
  const int w = __builtin_amdgcn_readfirstlane(tid >> 6);
  const int tg = w & 3, nh = w >> 2;
  const int g = l >> 4, lc = l & 15, q = (l >> 2) & 3, p = l & 3;
  STAMP(0, 0);
  const __amdgpu_buffer_rsrc_t rs = RSRC(ws);
  const char* xp;
  {
    int row = 8 * w + (l >> 3);
    xp = x + (size_t)(rb * 128 + row) * 1280 + (((l & 7) ^ (l >> 3)) << 4);
  }
  auto wsrc = [&](int row, int sl, int& ld) -> const char* {
    const char* wp;
    if (TYPE == 0) {
      if (sl >= 24) { wp = w1 + jj * 128 + (sl - 24) * 16; ld = 3456; }
      else { int s2 = sl < 11 ? (sl > 9 ? 9 : sl) : (sl > 20 ? 9 : sl - 11);
             wp = wqkv + (sl < 11 ? 1280 : 2560) + jj * 160 + s2 * 16; ld = 3840; }
    } else if (TYPE == 1) {
      if (sl >= 24) { wp = w1 + (8 + jj) * 128 + (sl - 24) * 16; ld = 3456; }
      else { int s2 = sl > 19 ? 19 : sl; wp = wqkv + jj * 320 + s2 * 16; ld = 3840; }
    } else {
      int L = 12 + 4 * jj + (sl >> 3); L = L > 26 ? 26 : L;
      wp = w1 + L * 128 + (sl & 7) * 16; ld = 3456;
    }
    return wp + (size_t)row * ld;
  };
  int ld;
  const char* wp;
  {
    int row = 2 * w + (l >> 5);
    wp = wsrc(row, (l & 31) ^ (((row & 3) << 1) | (((row >> 3) & 1) << 3)), ld);
  }
  unsigned tch = 0;
  if (CODE_PF && w == 0 && l < CODE_PF / 2048) {
    const unsigned long long a = (pc & ~255ull) + (unsigned)(((bid >> 3) * (CODE_PF / 2048) + l) * 64);
    asm volatile("global_load_dword %0, %1, off" : "+v"(tch) : "v"(a));
  }
  auto touch = [&]() {
    const int st = w < 7 ? 3 + w : 9;
    const int c8 = l & 7;
    const int sl = TYPE == 0 ? (c8 < 3 ? (c8 * 9 + 1) / 2 : (c8 < 6 ? 11 + ((c8 - 3) * 9 + 1) / 2 : 24 + (c8 - 6) * 4)) : (c8 * 4 + (c8 == 5 ? -1 : 0));
    int ld2;
    const char* a = wsrc(st * 64 + (rb & 7) * 8 + (l >> 3), sl, ld2);
    const char* b = x + (size_t)(rb * 128 + ((bid >> 3) & 3) * 32 + (l & 31)) * 1280 + st * 128;
    asm volatile("global_load_dword %0, %1, off\n\tglobal_load_dword %0, %2, off" : "+v"(tch) : "v"(a), "v"(b));
  };
  auto issue = [&](int s, int buf) {
    unsigned b = buf * STG;
    const char* xs = xp + s * 128;
    glds16(xs, b + w * 1024);
    glds16(xs + 64 * 1280, b + 8192 + w * 1024);
    const char* ws_ = wp + (size_t)(s * 64) * ld;
#pragma unroll
    for (int m = 0; m < 4; ++m) glds16(ws_ + (size_t)(16 * m) * ld, b + 16384 + (w + 8 * m) * 1024);
  };
  auto issue1 = [&](int s, int buf, int i) {
    unsigned b = buf * STG;
    if (i < 2) glds16(xp + s * 128 + i * 64 * 1280, b + i * 8192 + w * 1024);
    else glds16(wp + (size_t)(s * 64 + 16 * (i - 2)) * ld, b + 16384 + (w + 8 * (i - 2)) * 1024);
  };
  issue(0, 0); issue(1, 1); issue(2, 2);
  unsigned ta[NT], ca[2];
  {
    unsigned rowb = 16384 + (8 * g + q) * 512, sw = (q << 2) | ((g & 1) << 4);
    auto grp = [&](int t) -> int {
      if (TYPE == 0) {
        if (t < 2) return 5 * (2 * nh + t) + p;
        if (t == 2) return (p >> 1) * 22 + 10 * nh + 5 * (p & 1) + 4;
        if (t < 5) return 22 + 5 * (2 * nh + t - 3) + p;
        return 48 + 4 * (2 * nh + t - 5) + p;
      } else if (TYPE == 1) {
        if (t < 4) return 5 * (4 * nh + t) + p;
        if (t == 4) return 20 * nh + 5 * p + 4;
        return 48 + 4 * (2 * nh + t - 5) + p;
      } else return 32 * nh + 4 * t + p;
    };
#pragma unroll
    for (int t = 0; t < NT; ++t) ta[t] = rowb + ((grp(t) ^ sw) << 3);
#pragma unroll
    for (int c = 0; c < 2; ++c) {
      int t = 2 * tg + c; t = t >= NT ? NT - 1 : t;
      ca[c] = rowb + ((grp(t) ^ sw) << 3);
    }
  }
  unsigned xa[2][2];
#pragma unroll
  for (int tt = 0; tt < 2; ++tt)
#pragma unroll
    for (int k2 = 0; k2 < 2; ++k2) {
      int row = 32 * tg + 16 * (tt ^ nh) + lc;
      xa[tt][k2] = row * 128 + (((4 * k2 + g) ^ (row & 7)) << 4);
    }
  f32x4 acc[NT][2];
#pragma unroll
  for (int t = 0; t < NT; ++t) { acc[t][0] = (f32x4)(0.f); acc[t][1] = (f32x4)(0.f); }
  unsigned one2 = 0x3f803f80u;
  asm volatile("" : "+v"(one2));
  const int su0 = (rb * 4 + jj) * 6;
  auto side_issue = [&](int u, unsigned lb) {
    int ks = u / 5, cb = u - ks * 5;
    int v = 4 * w + (l >> 4);
    const char* src;
    if (ks < 54) src = w2 + (size_t)(32 * ks + v) * 1280;
    else {
      int k2 = ks - 54, hg = k2 / 5, kk = k2 - hg * 5, row;
      if (kk < 4) row = 20 * (8 * hg + 2 * kk + (v >> 4)) + (v & 15);
      else row = 20 * (8 * hg + 2 * ((v & 15) >> 2) + (v >> 4)) + 16 + (v & 3);
      src = wo + (size_t)row * 1280;
    }
    glds16(src + cb * 256 + (((l & 15) ^ ((v & 7) << 1)) << 4), lb + w * 1024);
  };

  constexpr int NF = NT + 2;
  unsigned fa[NF];
#pragma unroll
  for (int t = 0; t < NT; ++t) fa[t] = ta[t];
  fa[NT] = ca[0]; fa[NT + 1] = ca[1];
  u32x2 wl[2][NF], wh[2][NF];
  u32x4 xf[2][2];
#define RD(S, K2, I)                                                                                              \
  {                                                                                                               \
    if ((I) < 2) asm volatile("ds_read_b128 %0, %1" : "=v"(xf[S][(I) & 1]) : "v"(xa[(I) & 1][K2]));               \
    else if ((I) < 2 + 2 * NF) {                                                                                  \
      if (((I) & 1) == 0) asm volatile("ds_read_b64_tr_b16 %0, %1 offset:%2" : "=v"(wl[S][((I) - 2) >> 1 < NF ? ((I) - 2) >> 1 : 0]) : "v"(fa[((I) - 2) >> 1 < NF ? ((I) - 2) >> 1 : 0]), "i"((K2) * 16384)); \
      else asm volatile("ds_read_b64_tr_b16 %0, %1 offset:%2" : "=v"(wh[S][((I) - 2) >> 1 < NF ? ((I) - 2) >> 1 : 0]) : "v"(fa[((I) - 2) >> 1 < NF ? ((I) - 2) >> 1 : 0]), "i"((K2) * 16384 + 2048)); \
    }                                                                                                             \
  }
#define TIE(S)                                                                                                    \
  {                                                                                                               \
    asm volatile("" : "+v"(xf[S][0]), "+v"(xf[S][1]));                                                            \
    _Pragma("unroll") for (int t = 0; t < NF; ++t) asm volatile("" : "+v"(wl[S][t]), "+v"(wh[S][t]));             \
  }
#define NDBL 5
#define PRE1 0
  f32x4 mcs[2] = {(f32x4)(0.f), (f32x4)(0.f)}, mrs = (f32x4)(0.f), mrq = (f32x4)(0.f);
  const u32x4 ones = {one2, one2, one2, one2};
#define WFR(S, T) ((u32x4){wl[S][T][0], wl[S][T][1], wh[S][T][0], wh[S][T][1]})
#define CUM(I) ((I) + ((I) < NDBL ? (I) : NDBL) + ((I) > 3) + ((I) > 6) + ((I) > 9) + ((I) > 12))
#define PW(S, T) asm volatile("s_waitcnt lgkmcnt(12)" : "+v"(wl[S][T]), "+v"(wh[S][T]))
#define SLOT(S, K2N, DORD, I)                                                                                     \
  {                                                                                                               \
    if (DORD) { RD((S) ^ 1, K2N, CUM(I)); if ((I) < NDBL) RD((S) ^ 1, K2N, CUM(I) + 1); }                         \
    if ((I) == 3 || (I) == 6 || (I) == 9 || (I) == 12) {                                                          \
      __builtin_amdgcn_sched_barrier(0);                                                                          \
      if ((I) == 3) mrs = mfma32(ones, xf[S][0], mrs);                                                            \
      if ((I) == 6) mrq = mfma32(xf[S][0], xf[S][0], mrq);                                                        \
      if ((I) == 9) { if ((S) == 0) PW(S, NT); mcs[0] = mfma32(WFR(S, NT), ones, mcs[0]); }                       \
      if ((I) == 12) { if ((S) == 0) PW(S, NT + 1); mcs[1] = mfma32(WFR(S, NT + 1), ones, mcs[1]); }              \
      if (DORD) RD((S) ^ 1, K2N, CUM((I) + 1) - 1);                                                               \
    }                                                                                                             \
  }
#define KSTEP(S, K2N, DORD, GL)                                                                                   \
  {                                                                                                               \
    _Pragma("unroll") for (int t = 0; t < NT; ++t) {                                                              \
      if ((S) == 0) PW(S, t);                                                                                     \
      u32x4 wf = {wl[S][t][0], wl[S][t][1], wh[S][t][0], wh[S][t][1]};                                            \
      acc[t][0] = mfma32(wf, xf[S][0], acc[t][0]);                                                                \
      SLOT(S, K2N, DORD, 2 * t);                                                                                  \
      __builtin_amdgcn_sched_barrier(0);                                                                          \
      acc[t][1] = mfma32(wf, xf[S][1], acc[t][1]);                                                                \
      SLOT(S, K2N, DORD, 2 * t + 1);                                                                              \
      if ((GL) >= 0 && t >= 1 && t < 7) issue1(GL, (GL) % 3, t - 1);                                              \
      if ((GL) == -2 && TYPE == 1 && t >= 1 && t < 7) side_issue(su0 + t - 1 < 370 ? su0 + t - 1 : 369, STG + (t - 1) * 8192); \
      if ((GL) == -2 && TYPE == 0 && t == 1) { int c = tid < 200 ? tid : 199; if (w < 4) glds16(wphi + (size_t)jj * 3200 + c * 16, L_WPHI + w * 1024); } \
      __builtin_amdgcn_sched_barrier(0);                                                                          \
    }                                                                                                             \
  }
  if (PRE1 == 1) { touch(); WAITVM(14); } else WAITVM(12);
  BAR();
  STAMP(0, 1);
  if (PRE1 == 2) touch();
#pragma unroll
  for (int i = 0; i < 2 + 2 * NF; ++i) RD(0, 0, i);
#pragma unroll
  for (int s = 0; s < 10; ++s) {
    asm volatile("s_waitcnt lgkmcnt(12)" : "+v"(xf[0][0]), "+v"(xf[0][1]));
    __builtin_amdgcn_sched_barrier(0);
    KSTEP(0, 1, true, -1);
    WAITLGKM0();
    TIE(1);
    if (s < 9) {
      if (PRE1 && s < 2) WAITVM(8); else if (s < 8 || TYPE == 1) WAITVM(6); else if (TYPE == 0 && w < 4) WAITVM(1); else WAITVM(0);
      if (s == 2) asm volatile("" ::"v"(tch));
      BAR();
      const int d = (s % 3 == 2) ? -2 * STG : STG;
#pragma unroll
      for (int t = 0; t < NF; ++t) fa[t] += d;
      xa[0][0] += d; xa[0][1] += d; xa[1][0] += d; xa[1][1] += d;
    }
    __builtin_amdgcn_sched_barrier(0);
    KSTEP(1, 0, s < 9, (s <= 6 ? s + 3 : (s == 7 ? -2 : -1)));
  }
  STAMP(0, 2);
  {
    if (lc < 2) *(LDSP(f32x4))(L_CS + nh * 512 + (2 * tg + lc) * 64 + g * 16) = lc ? mcs[1] : mcs[0];
    float sm = mrs[0], sq = p == 0 ? mrq[0] : (p == 1 ? mrq[1] : (p == 2 ? mrq[2] : mrq[3]));
    float mean_ = sm * (1.0f / 640.0f);
    float var = fmaxf(sq * (1.0f / 640.0f) - mean_ * mean_, 0.f) + 1e-5f;
    f32x2 st = {mean_, __builtin_amdgcn_rsqf(var)};
    if (g == q) *(LDSP(f32x2))(L_ST + (32 * tg + 16 * nh + lc) * 8) = st;
  }
  WAITVM(0);
  WAITLGKM0();
  BAR();
  float mean[2], rstd[2];
#pragma unroll
  for (int tt = 0; tt < 2; ++tt) {
    f32x2 st = *(LDSP(f32x2))(L_ST + (32 * tg + 16 * (tt ^ nh) + lc) * 8);
    mean[tt] = st[0]; rstd[tt] = st[1];
  }
#pragma unroll
  for (int t = 0; t < NT; ++t) {
    f32x4 c4 = *(LDSP(f32x4))(L_CS + nh * 512 + t * 64 + g * 16);
#pragma unroll
    for (int tt = 0; tt < 2; ++tt) {
      f32x4 mm = {-mean[tt], -mean[tt], -mean[tt], -mean[tt]};
      acc[t][tt] = __builtin_elementwise_fma(mm, c4, acc[t][tt]);
    }
  }
  STAMP(0, 3);
  if (TYPE == 1) {
    unsigned a = STG + (4 * g + q) * 256 + (((2 * w + (p >> 1)) ^ (((4 * g + q) & 7) << 1)) << 4) + (p & 1) * 8;
#pragma unroll
    for (int i = 0; i < 6; ++i) {
      if (su0 + i < 370) {
        int u = su0 + i, ks = u / 5, cb = u - ks * 5, ct = cb * 8 + w, nb = ct / 5, ci = ct - nb * 5;
        u32x4 f = trd2(a + i * 8192, a + i * 8192 + 4096);
        ST16(WS_BF + ((nb * 74 + ks) * 5 + ci) * 1024 + l * 16, f);
      }
    }
  }
  {
    constexpr int T0 = TYPE == 2 ? 0 : 5;
#pragma unroll
    for (int t = T0; t < NT; t += 2) {
      int ks;
      if (TYPE == 0) ks = 2 * jj + nh;
      else if (TYPE == 1) ks = 2 * (8 + jj) + nh;
      else ks = 2 * (12 + 4 * jj) + 4 * nh + (t >> 1);
      if (ks < 54) {
#pragma unroll
        for (int tt = 0; tt < 2; ++tt) {
          const float c1 = rstd[tt] * -1.4426950408889634f, irs = __builtin_amdgcn_rcpf(rstd[tt]);
          u32x4 o = {silu2(acc[t][tt][0], acc[t][tt][1], c1, irs), silu2(acc[t][tt][2], acc[t][tt][3], c1, irs),
                     silu2(acc[t + 1][tt][0], acc[t + 1][tt][1], c1, irs), silu2(acc[t + 1][tt][2], acc[t + 1][tt][3], c1, irs)};
          int gtt = rb * 8 + 2 * tg + (tt ^ nh);
          ST16(WS_HF + (gtt * 54 + ks) * 1024 + l * 16, o);
        }
      }
    }
  }
  STAMP(0, 4);
  if (TYPE == 2) return;
  if (TYPE == 1) {
    const int tp = rb * 4 + tg;
#pragma unroll
    for (int t = 0; t < 5; ++t) {
      f32x4 v0 = acc[t][0] * rstd[0], v1 = acc[t][1] * rstd[1];
      u32x2 a = {pk2(v0[0], v0[1]), pk2(v0[2], v0[3])}, b = {pk2(v1[0], v1[1]), pk2(v1[2], v1[3])};
      u32x4 o = nh ? (u32x4){b[0], b[1], a[0], a[1]} : (u32x4){a[0], a[1], b[0], b[1]};
      int dst = t < 4 ? (int)WS_QF + ((jj * 8 + nh * 4 + t) * 64 + tp) * 1024 : (int)WS_QX + ((jj * 2 + nh) * 64 + tp) * 1024;
      ST16(dst + l * 16, o);
    }
    {
      const u32x4 sen = {0xffffffffu, 0xffffffffu, 0xffffffffu, 0xffffffffu};
      int dst = (int)WS_OF + (rb * 4 + jj) * 40960 + tid * 16;
#pragma unroll
      for (int i = 0; i < 5; ++i) ST16(dst + i * 8192, sen);
    }
    STAMP(0, 5);
    return;
  }
  u32x2 xb[2];
#pragma unroll
  for (int tt = 0; tt < 2; ++tt) { xb[tt][0] = pk2(acc[2][tt][0], acc[2][tt][1]); xb[tt][1] = pk2(acc[2][tt][2], acc[2][tt][3]); }
#pragma unroll
  for (int hh = 0; hh < 2; ++hh) {
    const int hl = 2 * nh + hh;
    unsigned wb = L_WPHI + hl * 800;
    u32x4 a0 = trd2(wb + (4 * g + q) * 40 + p * 8, wb + (16 + q) * 40 + p * 8);
    u32x4 a1;
    {
      unsigned b1 = wb + 4 * g * 40 + 32 + q * 2, b2 = wb + 640 + 32 + q * 2;
      a1[0] = (unsigned)*(LDSP(unsigned short))(b1) | ((unsigned)*(LDSP(unsigned short))(b1 + 40) << 16);
      a1[1] = (unsigned)*(LDSP(unsigned short))(b1 + 80) | ((unsigned)*(LDSP(unsigned short))(b1 + 120) << 16);
      a1[2] = (unsigned)*(LDSP(unsigned short))(b2) | ((unsigned)*(LDSP(unsigned short))(b2 + 40) << 16);
      a1[3] = (unsigned)*(LDSP(unsigned short))(b2 + 80) | ((unsigned)*(LDSP(unsigned short))(b2 + 120) << 16);
    }
    if (rb == 0 && tg == 0) {
      int wf = (int)WS_WF + (jj * 4 + hl) * 2048 + l * 16;
      ST16(wf, a0); ST16(wf + 1024, a1);
    }
    if (g != hh) { a0[2] = 0; a0[3] = 0; a1[2] = 0; a1[3] = 0; }
#pragma unroll
    for (int tt = 0; tt < 2; ++tt) {
      u32x4 kb = {pk2(acc[hh][tt][0], acc[hh][tt][1]), pk2(acc[hh][tt][2], acc[hh][tt][3]), xb[tt][0], xb[tt][1]};
      f32x4 pm = mfma32(a0, kb, (f32x4)(0.f));
      f32x4 px = mfma32(a1, kb, (f32x4)(0.f));
      u32x4 mv; unsigned ev;
      hedge(pm, px[0], rstd[tt] * 1.4426950408889634f, mv, ev);
      unsigned ib = (nh ? 0 : 2 * STG) + hh * KVI + (32 * tg + 16 * (tt ^ nh) + lc) * RS;
      *(LDSP(u32x4))(ib + g * 16) = mv;
      *(LDSP(unsigned))(ib + 64 + g * 4) = ev;
      f32x4 v = acc[3 + hh][tt] * rstd[tt];
      u32x2 vv = {pk2(v[0], v[1]), pk2(v[2], v[3])};
      *(LDSP(u32x2))(ib + 80 + g * 8) = vv;
    }
  }
  STAMP(0, 5);
  {
#pragma unroll
    for (int tt = 0; tt < 2; ++tt) {
      unsigned ib = (nh ? 0 : 2 * STG) + (g & 1) * KVI + (32 * tg + 16 * (tt ^ nh) + lc) * RS + (g >= 2 ? 112 : 120);
      f32x4 v = acc[2][tt] * rstd[tt];
      u32x2 vv = {pk2(v[0], v[1]), pk2(v[2], v[3])};
      if (g < 2) { vv[0] = 0x00003f80u; vv[1] = 0u; }
      *(LDSP(u32x2))(ib) = vv;
    }
    WAITVM(0);
    WAITLGKM0();
    BAR();
    if (tid < 2) flag_set(ws, 2048 + tid * 256 + rb * 16 + (jj >> 1) * 4 + (jj & 1));
    const int hl = w >> 1, dt = w & 1;
    unsigned ib = (hl < 2 ? 2 * STG : 0) + (hl & 1) * KVI + (4 * g + q) * RS + p * 8;
    f32x4 kv0 = (f32x4)(0.f), kv1 = (f32x4)(0.f), kv2 = (f32x4)(0.f);
#pragma unroll
    for (int k = 0; k < 4; ++k) {
      unsigned a = ib + k * 32 * RS;
      u32x4 bv = trd2(a + 80 + dt * 32, a + 80 + dt * 32 + 16 * RS);
      kv0 = mfma32(trd2(a, a + 16 * RS), bv, kv0);
      kv1 = mfma32(trd2(a + 32, a + 32 + 16 * RS), bv, kv1);
      kv2 = mfma32(trd2(a + 64, a + 64 + 16 * RS), bv, kv2);
    }
    if (dt == 0 || lc < 5) {
      int col = 16 * dt + lc;
      int o = (int)WS_KVP + ((jj * 4 + hl) * 16 + rb) * 3360 + col * 160 + g * 16;
      ST16(o, kv0);
      ST16(o + 64, kv1);
      if (g < 2) ST16(o + 128, kv2);
    }
    STAMP(0, 6);
  }
}

constexpr int L2_G = 102400;
constexpr int L2_S = 97280;
constexpr int K2_LDS = 163840;
#define PF 3
#define HP 2
#define SLP2 4
#define WOD 58
#define WB 90
#define PRE 0
#define PRE0 6
#define SLP3 2
#define GB 0
#define AUX 16
#define ROT 0
#define ROTX 0
#define MAP 1
#define SLP0 8
#define SLP1 4
__device__ __forceinline__ void lds_wait(LDSP(unsigned) f, unsigned v) {
  for (int sp = 0; sp < (1 << 22); ++sp) {
    if (__hip_atomic_load(f, __ATOMIC_RELAXED, __HIP_MEMORY_SCOPE_WORKGROUP) >= v) break;
    __builtin_amdgcn_s_sleep(1);
  }
  asm volatile("" ::: "memory");
}
__global__ __launch_bounds__(512, 2) void kf(const char* __restrict__ x, const char* __restrict__ wqkv, const char* __restrict__ wphi,
                                             const char* __restrict__ wo, const char* __restrict__ w1, const char* __restrict__ w2,
                                             char* __restrict__ ws, char* __restrict__ out) {
  unsigned long long pc;
  asm volatile("s_getpc_b64 %0" : "=s"(pc));
  const int bid = blockIdx.x, xcd = bid & 7, idx = bid >> 3;
  {
    const int rb = (xcd >> 2) * 8 + (idx >> 2), j = xcd & 3, ty = idx & 3;
    if (ty < 2) k1_body<0>(x, wqkv, wphi, wo, w1, w2, ws, rb, 2 * j + ty, bid, pc);
    else if (ty == 2) k1_body<1>(x, wqkv, wphi, wo, w1, w2, ws, rb, j, bid, pc);
    else k1_body<2>(x, wqkv, wphi, wo, w1, w2, ws, rb, j, bid, pc);
    __builtin_amdgcn_s_waitcnt(0);
    WAITVM(0);
    BAR();
    const int t = threadIdx.x;
    if (ty < 2) { if (t < 4) flag_set(ws, 2560 + t * 128 + (2 * j + ty) * 16 + rb); }
    else if (t < 2) flag_set(ws, 2048 + t * 256 + rb * 16 + j * 4 + ty);
    else if (ty == 2 && t < 34) flag_set(ws, (t - 2) * 64 + rb * 4 + j);
  }
  const __amdgpu_buffer_rsrc_t rs = RSRC(ws);
  const int mb = 8 * (xcd >> 1) + (idx >> 2), nb = 4 * (xcd & 1) + ((idx + (idx >> 2)) & 3);
  const int tgp = idx >> 2;
  const int tid = threadIdx.x, l = tid & 63;
  const int w = __builtin_amdgcn_readfirstlane(tid >> 6);
  const int g = l >> 4, lc = l & 15;
  const unsigned lo = l * 16;
  STAMP(1, 0);
  LDSP(unsigned) flg = (LDSP(unsigned))(L2_S + 2048);
  if (w < 4) {
    __builtin_amdgcn_s_setprio(HP);
    const int head = xcd * 4 + (idx & 3);
    if (tid < 8) flg[tid] = 0u;
    WAITLGKM0();
    BAR();
    lds_wait(flg + 1, 1u);
    u32x4 qf[2], qx[2];
#pragma unroll
    for (int i = 0; i < 2; ++i) {
      int tp = tgp * 8 + w * 2 + i;
      qf[i] = LD16(WS_QF + (head * 64 + tp) * 1024 + lo);
      qx[i] = LD16(WS_QX + ((head >> 2) * 64 + tp) * 1024 + lo);
    }
    if (w == 0) {
      const unsigned long long hi = tag_hi();
      for (int sp = 0; sp < (1 << 18); ++sp) {
        bool ok = flag_ok(ws, 2560 + (idx & 3) * 128 + xcd * 16 + lc, hi);
        if (__builtin_amdgcn_ballot_w64(!ok) == 0ull) break;
        __builtin_amdgcn_s_sleep(4);
      }
      if (l == 0) __hip_atomic_store(flg + 4, 1u, __ATOMIC_RELAXED, __HIP_MEMORY_SCOPE_WORKGROUP);
    } else lds_wait(flg + 4, 1u);
    asm volatile("" ::: "memory");
    STAMP(1, 7);
    u32x4 a0 = LD16(WS_WF + head * 2048 + lo), a1 = LD16(WS_WF + head * 2048 + 1024 + lo);
    f32x4 part[16];
    const int kp = (int)WS_KVP + head * 16 * 3360 + (tid < 210 ? tid : 209) * 16;
#pragma unroll
    for (int i = 0; i < 16; ++i) part[i] = __builtin_bit_cast(f32x4, LD16(kp + i * 3360));
    __builtin_amdgcn_sched_barrier(0);
    if (g != (head & 3)) { a0[2] = 0; a0[3] = 0; a1[2] = 0; a1[3] = 0; }
    u32x4 pq[4]; unsigned px[4];
#pragma unroll
    for (int i = 0; i < 4; ++i) {
      u32x4 kb = {qf[i >> 1][2 * (i & 1)], qf[i >> 1][2 * (i & 1) + 1], qx[i >> 1][2 * (i & 1)], qx[i >> 1][2 * (i & 1) + 1]};
      f32x4 pm = mfma32(a0, kb, (f32x4)(0.f));
      f32x4 pe = mfma32(a1, kb, (f32x4)(0.f));
      hedge(pm, pe[0], 1.4426950408889634f, pq[i], px[i]);
    }
    __builtin_amdgcn_sched_barrier(0);
    f32x4 s = ((part[0] + part[1]) + (part[2] + part[3])) + ((part[4] + part[5]) + (part[6] + part[7])) +
              (((part[8] + part[9]) + (part[10] + part[11])) + ((part[12] + part[13]) + (part[14] + part[15])));
    u32x2 sb = {pk2(s[0], s[1]), pk2(s[2], s[3])};
    if (tid < 210) *(LDSP(u32x2))(L2_S + tid * 8) = sb;
    WAITLGKM0();
    if (l == 0) __hip_atomic_fetch_add(flg, 1u, __ATOMIC_RELAXED, __HIP_MEMORY_SCOPE_WORKGROUP);
    lds_wait(flg, 4u);
    STAMP(1, 1);
    {
      const int c1 = 16 + (lc & 3);
      u32x4 b0 = lds128(L2_S + lc * 80 + g * 16), b1 = lds128(L2_S + c1 * 80 + g * 16), b2 = lds128(L2_S + 1600 + g * 16);
      u32x4 e0 = {*(LDSP(unsigned))(L2_S + lc * 80 + 64 + g * 4), 0u, 0u, 0u}, e1 = {*(LDSP(unsigned))(L2_S + c1 * 80 + 64 + g * 4), 0u, 0u, 0u},
            e2 = {*(LDSP(unsigned))(L2_S + 1600 + 64 + g * 4), 0u, 0u, 0u};
      unsigned long long oe = 0;
#pragma unroll
      for (int i = 0; i < 4; ++i) {
        u32x4 pe = {px[i], 0u, 0u, 0u};
        f32x4 d2 = mfma32(b2, pq[i], (f32x4)(0.f));
        f32x4 d0 = mfma32(b0, pq[i], (f32x4)(0.f));
        f32x4 d1 = mfma32(b1, pq[i], (f32x4)(0.f));
        d2 = mfma32(e2, pe, d2);
        d0 = mfma32(e0, pe, d0);
        d1 = mfma32(e1, pe, d1);
        float inv = __builtin_amdgcn_rcpf(d2[0] + 1e-6f);
        unsigned long long ov = (unsigned long long)pk2(d0[0] * inv, d0[1] * inv) | ((unsigned long long)pk2(d0[2] * inv, d0[3] * inv) << 32);
        unsigned long long ev = (unsigned long long)pk2(d1[0] * inv, d1[1] * inv) | ((unsigned long long)pk2(d1[2] * inv, d1[3] * inv) << 32);
        if (g == i) oe = ev;
        int gtt = tgp * 16 + w * 4 + i;
        __hip_atomic_store((unsigned long long*)(ws + WS_OF + ((size_t)head * 128 + gtt) * 512 + l * 8), ov, __ATOMIC_RELAXED, __HIP_MEMORY_SCOPE_AGENT);
      }
      __hip_atomic_store((unsigned long long*)(ws + WS_OX + ((size_t)((tgp * 16 + w * 4 + g) * 4 + (head >> 3)) * 2 + (head & 1)) * 512 + ((head & 7) >> 1) * 128 + lc * 8),
                         oe, __ATOMIC_RELAXED, __HIP_MEMORY_SCOPE_AGENT);
    }
    STAMP(1, 2);
    __builtin_amdgcn_sched_barrier(0);
    u32x4 bl[5];
#pragma unroll
    for (int c = 0; c < 5; ++c) bl[c] = LD16(WS_BF + (nb * 74 + 73) * 5120 + c * 1024 + lo);
    const size_t orow = (size_t)(mb * 64 + w * 16 + lc) * 1280 + nb * 160 + g * 8;
    u32x2 xv[5];
#pragma unroll
    for (int c = 0; c < 5; ++c) xv[c] = *(const u32x2*)(x + orow + c * 32);
    __builtin_amdgcn_sched_barrier(0);
    unsigned long long om[32], ox[8];
    {
      const char* op = ws + WS_OF + (size_t)(mb * 4 + w) * 512 + l * 8;
      const char* oxp = ws + WS_OX + (size_t)(mb * 4 + w) * 4096 + l * 8;
      unsigned need = 15u;
      __builtin_amdgcn_s_sleep(SLP0);
      for (int sp = 0; sp < (1 << 16); ++sp) {
#pragma unroll
        for (int hg = 0; hg < 4; ++hg)
          if (need & (1u << hg)) {
#pragma unroll
            for (int j = 0; j < 8; ++j)
              om[8 * hg + j] = __hip_atomic_load((const unsigned long long*)(op + (size_t)(8 * hg + j) * 65536), __ATOMIC_RELAXED, __HIP_MEMORY_SCOPE_AGENT);
#pragma unroll
            for (int j = 0; j < 2; ++j)
              ox[2 * hg + j] = __hip_atomic_load((const unsigned long long*)(oxp + (2 * hg + j) * 512), __ATOMIC_RELAXED, __HIP_MEMORY_SCOPE_AGENT);
          }
#pragma unroll
        for (int hg = 0; hg < 4; ++hg)
          if (need & (1u << hg)) {
            unsigned m = max(max((unsigned)ox[2 * hg], (unsigned)(ox[2 * hg] >> 32)), max((unsigned)ox[2 * hg + 1], (unsigned)(ox[2 * hg + 1] >> 32)));
#pragma unroll
            for (int j = 0; j < 8; ++j) m = max(max(m, (unsigned)om[8 * hg + j]), (unsigned)(om[8 * hg + j] >> 32));
            if (__builtin_amdgcn_ballot_w64(m == 0xffffffffu) == 0ull) need &= ~(1u << hg);
          }
        if (need == 0u) break;
        __builtin_amdgcn_s_sleep(SLP1);
      }
    }
    STAMP(1, 3);
    __builtin_amdgcn_sched_barrier(0);
    lds_wait(flg + 3, 4u);
    u32x4 wf[3][5];
#pragma unroll
    for (int k = 0; k < 3; ++k)
#pragma unroll
      for (int c = 0; c < 5; ++c) wf[k][c] = lds128((k * 5 + c) * 1024 + lo);
    STAMP(1, 6);
    f32x4 acc[5];
#pragma unroll
    for (int c = 0; c < 5; ++c) acc[c] = (f32x4)(0.f);
#pragma unroll
    for (int k = 0; k < 20; ++k) {
      const int hg = k / 5, kk = k % 5;
      const unsigned long long f0 = kk < 4 ? om[8 * hg + 2 * (kk & 3)] : ox[2 * hg], f1 = kk < 4 ? om[8 * hg + 2 * (kk & 3) + 1] : ox[2 * hg + 1];
      u32x4 af = {(unsigned)f0, (unsigned)(f0 >> 32), (unsigned)f1, (unsigned)(f1 >> 32)};
#pragma unroll
      for (int c = 0; c < 5; ++c) {
        acc[c] = mfma32(k < 19 ? wf[k % 3][c] : bl[c], af, acc[c]);
        if (k + 3 < 19) wf[k % 3][c] = lds128(((k + 3) * 5 + c) * 1024 + lo);
        __builtin_amdgcn_sched_barrier(0);
      }
    }
    __builtin_amdgcn_s_setprio(0);
    if (l == 0) __hip_atomic_fetch_add(flg + 2, 1u, __ATOMIC_RELAXED, __HIP_MEMORY_SCOPE_WORKGROUP);
    STAMP(1, 4);
    BAR();
#pragma unroll
    for (int c = 0; c < 5; ++c) {
      unsigned a = L2_G + (w * 5 + c) * 1024 + lo;
      f32x4 gv = (*(LDSP(f32x4))(a) + *(LDSP(f32x4))(a + 20480)) + (*(LDSP(f32x4))(a + 40960) + *(LDSP(f32x4))(a - L2_G));
      float o[4];
#pragma unroll
      for (int r = 0; r < 4; ++r) {
        float xf = __builtin_bit_cast(float, (r & 1) ? (xv[c][r >> 1] & 0xffff0000u) : (xv[c][r >> 1] << 16));
        o[r] = __builtin_fmaf(acc[c][r], silu_f(gv[r]), xf);
      }
      u32x2 ov = {pk2(o[0], o[1]), pk2(o[2], o[3])};
      *(u32x2*)(out + orow + c * 32) = ov;
    }
    STAMP(1, 5);
  } else {
    f32x4 acc[4][5];
#pragma unroll
    for (int i = 0; i < 4; ++i)
#pragma unroll
      for (int c = 0; c < 5; ++c) acc[i][c] = (f32x4)(0.f);
    const int k0 = (w - 4) * 14 - (w == 7 ? 1 : 0);
    const int nk = w < 6 ? 14 : 13;
    const int rot = ROT ? (2 * (idx & 7) + 7 * ((idx >> 3) & 1) + 3 * (idx >> 4) + ROTX * xcd) % 13 : 0;
    auto kidx = [&](int k) { int kn = (k < nk ? k : nk - 1) + rot; return kn >= nk ? kn - nk : kn; };
    const int ap = (int)WS_HF + (mb * 4 * 54 + k0) * 1024, bp = (int)WS_BF + (nb * 74 + k0) * 5120;
#define BLD(OFF) __builtin_bit_cast(u32x4, __builtin_amdgcn_raw_buffer_load_b128(rs, lo, (OFF), AUX))
    u32x4 af[PF][4], bfr[PF][5];
    const int f0 = w == 7 ? 71 : (w - 4) * 24;
    const char* wp = ws + WS_BF + (size_t)(nb * 74 + 54) * 5120 + f0 * 1024 + lo;
    BAR();
    if (w == 4) {
      const unsigned long long hi = tag_hi();
      for (int sp = 0; sp < (1 << 18); ++sp) {
        bool ok = flag_ok(ws, idx * 64 + l, hi) & flag_ok(ws, 2048 + (mb & 1) * 256 + (mb >> 1) * 16 + lc, hi);
        if (__builtin_amdgcn_ballot_w64(!ok) == 0ull) break;
        __builtin_amdgcn_s_sleep(4);
      }
      if (l == 0) __hip_atomic_store(flg + 1, 1u, __ATOMIC_RELAXED, __HIP_MEMORY_SCOPE_WORKGROUP);
    } else lds_wait(flg + 1, 1u);
    asm volatile("" ::: "memory");
    STAMP(1, 7);
#pragma unroll
    for (int i0 = 0; i0 < 6; ++i0) {
      const int i = ROT ? (i0 + (idx & 7)) % 6 : i0;
      const char* a = wp + i * 4096;
      asm volatile("s_mov_b32 m0, %1\n\ts_nop 0\n\tglobal_load_lds_dwordx4 %0, off sc1\n\tglobal_load_lds_dwordx4 %0, off offset:1024 sc1\n\t"
                   "global_load_lds_dwordx4 %0, off offset:2048 sc1\n\tglobal_load_lds_dwordx4 %0, off offset:3072 sc1"
                   ::"v"(a), "s"((f0 + 4 * i) * 1024) : "memory");
    }
#pragma unroll
    for (int k = 0; k < PF; ++k) {
      const int kn = kidx(k);
#pragma unroll
      for (int c = 0; c < 5; ++c) bfr[k][c] = BLD(bp + kn * 5120 + c * 1024);
#pragma unroll
      for (int i = 0; i < 4; ++i) af[k][i] = BLD(ap + i * 55296 + kn * 1024);
    }
    __builtin_amdgcn_sched_barrier(0);
#pragma unroll
    for (int k = 0; k < 14; ++k) {
      if (k == 13 && w >= 6) {
#pragma unroll
        for (int i = 0; i < 4; ++i) af[k % PF][i] = (u32x4)(0u);
      }
#pragma unroll
      for (int i = 0; i < 4; ++i)
#pragma unroll
        for (int c = 0; c < 5; ++c) acc[i][c] = mfma32(bfr[k % PF][c], af[k % PF][i], acc[i][c]);
      __builtin_amdgcn_sched_barrier(0);
      if (k == 0 && l == 0) __hip_atomic_fetch_add(flg + 3, 1u, __ATOMIC_RELAXED, __HIP_MEMORY_SCOPE_WORKGROUP);
      if (k + PF < 14) {
        const int kn = kidx(k + PF);
#pragma unroll
        for (int c = 0; c < 5; ++c) bfr[k % PF][c] = BLD(bp + kn * 5120 + c * 1024);
#pragma unroll
        for (int i = 0; i < 4; ++i) af[k % PF][i] = BLD(ap + i * 55296 + kn * 1024);
      }
      __builtin_amdgcn_sched_barrier(0);
    }
    STAMP(1, 4);
    if (w == 7) lds_wait(flg + 2, 4u);
    unsigned base = (w == 7 ? 0 : L2_G + (w - 4) * 20480) + lo;
#pragma unroll
    for (int i = 0; i < 4; ++i)
#pragma unroll
      for (int c = 0; c < 5; ++c) *(LDSP(f32x4))(base + (i * 5 + c) * 1024) = acc[i][c];
    WAITLGKM0();
    BAR();
  }
}

extern "C" void kernel_launch(void* const* d_in, const int* in_sizes, int n_in, void* d_out, int out_size, void* d_ws, size_t ws_size,
                              hipStream_t stream) {
  (void)in_sizes; (void)n_in; (void)out_size; (void)ws_size;
  hipFuncSetAttribute((const void*)kf, hipFuncAttributeMaxDynamicSharedMemorySize, K2_LDS);
  kf<<<256, 512, K2_LDS, stream>>>((const char*)d_in[0], (const char*)d_in[1], (const char*)d_in[2], (const char*)d_in[3],
                                   (const char*)d_in[5], (const char*)d_in[6], (char*)d_ws, (char*)d_out);
}
```

```cpp
#include <hip/hip_runtime.h>
#include <stdint.h>

typedef __attribute__((ext_vector_type(8))) __bf16 bf16x8;
typedef __attribute__((ext_vector_type(2))) __bf16 bf16x2;
typedef __attribute__((ext_vector_type(4))) short s16x4;
typedef __attribute__((ext_vector_type(4))) float f32x4;
typedef __attribute__((ext_vector_type(2))) float f32x2;
typedef __attribute__((ext_vector_type(4))) unsigned u32x4;
typedef __attribute__((ext_vector_type(2))) unsigned u32x2;
#define LDSP(T) __attribute__((address_space(3))) T*
#define GLBP(T) __attribute__((address_space(1))) T*

constexpr size_t WS_HF  = 0;
constexpr size_t WS_OF  = WS_HF + 128ull * 54 * 1024;
constexpr size_t WS_OX  = WS_OF + 32ull * 128 * 512;
constexpr size_t WS_BF  = WS_OF + 128ull * 20 * 1024;
constexpr size_t WS_QF  = WS_BF + 8ull * 74 * 5 * 1024;
constexpr size_t WS_QX  = WS_QF + 32ull * 64 * 1024;
constexpr size_t WS_KVP = WS_QX + 8ull * 64 * 1024;
constexpr size_t WS_WF  = WS_KVP + 32ull * 16 * 3360;
constexpr size_t WS_CNT = WS_WF + 32ull * 2048;

#define STAMP(K, I)
constexpr int STG = 49152;
constexpr int L_WPHI = 3 * STG;
constexpr int L_CS = L_WPHI + 7168;
constexpr int L_ST = L_CS + 1024;
constexpr int K1_LDS = L_ST + 1024;
constexpr int RS = 144;
constexpr int KVI = 128 * RS;

__device__ __forceinline__ unsigned pk2(float a, float b) {
  f32x2 v = {a, b};
  bf16x2 r = __builtin_convertvector(v, bf16x2);
  return __builtin_bit_cast(unsigned, r);
}
__device__ __forceinline__ f32x4 mfma32(u32x4 a, u32x4 b, f32x4 c) {
  return __builtin_amdgcn_mfma_f32_16x16x32_bf16(__builtin_bit_cast(bf16x8, a), __builtin_bit_cast(bf16x8, b), c, 0, 0, 0);
}
__device__ __forceinline__ f32x4 mfma16(u32x2 a, u32x2 b, f32x4 c) {
  return __builtin_amdgcn_mfma_f32_16x16x16bf16_1k(__builtin_bit_cast(s16x4, a), __builtin_bit_cast(s16x4, b), c, 0, 0, 0);
}
__device__ __forceinline__ u32x2 trd(unsigned addr) {
  s16x4 v = __builtin_amdgcn_ds_read_tr16_b64_v4i16((LDSP(s16x4))addr);
  return __builtin_bit_cast(u32x2, v);
}
__device__ __forceinline__ u32x4 trd2(unsigned a0, unsigned a1) {
  u32x2 lo = trd(a0), hi = trd(a1);
  u32x4 r = {lo.x, lo.y, hi.x, hi.y};
  return r;
}
__device__ __forceinline__ u32x4 lds128(unsigned addr) { return *(LDSP(u32x4))addr; }
__device__ __forceinline__ void glds16(const char* g, unsigned ldsbase) {
  __builtin_amdgcn_global_load_lds((GLBP(const void))g, (LDSP(void))ldsbase, 16, 0, 0);
}
__device__ __forceinline__ float asf(unsigned u) { return __builtin_bit_cast(float, u); }
__device__ __forceinline__ unsigned asu(float f) { return __builtin_bit_cast(unsigned, f); }
__device__ __forceinline__ float gsum(float a) {
  auto r = __builtin_amdgcn_permlane16_swap(asu(a), asu(a), false, false);
  float m = asf(r[0]) + asf(r[1]);
  auto r2 = __builtin_amdgcn_permlane32_swap(asu(m), asu(m), false, false);
  return asf(r2[0]) + asf(r2[1]);
}
__device__ __forceinline__ float dot2(unsigned a, unsigned b, float c) {
  asm volatile("v_dot2c_f32_bf16 %0, %1, %2" : "+v"(c) : "v"(a), "v"(b));
  return c;
}
__device__ __forceinline__ float silu_f(float v) {
  float e = __builtin_amdgcn_exp2f(-1.4426950408889634f * v);
  return v * __builtin_amdgcn_rcpf(1.0f + e);
}
#define CODE_PF 57344
#define RSRC(ws) __builtin_amdgcn_make_buffer_rsrc(ws, 0, (int)WS_CNT, 0x00020000)
#define ST16(OFF, V) __builtin_amdgcn_raw_buffer_store_b128(__builtin_bit_cast(u32x4, V), rs, (int)(OFF), 0, 16)
#define LD16(OFF) __builtin_bit_cast(u32x4, __builtin_amdgcn_raw_buffer_load_b128(rs, (int)(OFF), 0, 16))
extern "C" __device__ __attribute__((const)) unsigned long long launch_seq() __asm("llvm.amdgcn.dispatch.id");
__device__ __forceinline__ unsigned long long tag_hi() {
  const unsigned long long pk = (unsigned long long)__builtin_amdgcn_dispatch_ptr();
  return (((launch_seq() + 0x51ull) * 0x9E3779B97F4A7C15ull ^ 0xA5C3D2E1F0B49687ull) + pk * 0xD6E8FEB86659FD93ull) & ~0xffffull;
}
__device__ __forceinline__ unsigned tag_lo() { return (unsigned)(__builtin_amdgcn_s_memrealtime() >> 20) & 0xffffu; }
__device__ __forceinline__ void flag_set(char* ws, int i) {
  __hip_atomic_store((unsigned long long*)(ws + WS_CNT) + i, tag_hi() | tag_lo(), __ATOMIC_RELAXED, __HIP_MEMORY_SCOPE_AGENT);
}
__device__ __forceinline__ bool flag_ok(const char* ws, int i, unsigned long long hi) {
  unsigned long long v = __hip_atomic_load((const unsigned long long*)(ws + WS_CNT) + i, __ATOMIC_RELAXED, __HIP_MEMORY_SCOPE_AGENT);
  return (v & ~0xffffull) == hi && ((tag_lo() - (unsigned)v) & 0xffffu) <= 1u;
}
#define WAITVM_(n) asm volatile("s_waitcnt vmcnt(" #n ")" ::: "memory")
#define WAITVM(n) WAITVM_(n)
#define WAITLGKM0() asm volatile("s_waitcnt lgkmcnt(0)" ::: "memory")
#define BAR() __builtin_amdgcn_s_barrier()

__device__ __forceinline__ void hedge(f32x4 p, float xe, float sc, u32x4& mainv, unsigned& extv) {
  float mx = fmaxf(fmaxf(fmaxf(p[0], p[1]), p[2]), fmaxf(p[3], xe));
  float mn = fminf(fminf(fminf(p[0], p[1]), p[2]), fminf(p[3], xe));
  auto r = __builtin_amdgcn_permlane16_swap(asu(mx), asu(-mn), false, false);
  float m = fmaxf(asf(r[0]), asf(r[1]));
  r = __builtin_amdgcn_permlane32_swap(asu(m), asu(m), false, false);
  m = fmaxf(asf(r[0]), asf(r[1])) * -sc;
  r = __builtin_amdgcn_permlane16_swap(asu(m), asu(m), false, false);
  const f32x2 bb = {asf(r[0]), asf(r[1])}, ss = {sc, -sc};
  f32x2 e[5];
#pragma unroll
  for (int i = 0; i < 5; ++i) {
    float v = i < 4 ? p[i & 3] : xe;
    f32x2 vv = {v, v};
    f32x2 t = __builtin_elementwise_fma(vv, ss, bb);
    e[i][0] = __builtin_amdgcn_exp2f(t[0]);
    e[i][1] = __builtin_amdgcn_exp2f(t[1]);
  }
  f32x2 s = ((e[0] + e[1]) + (e[2] + e[3])) + e[4];
  r = __builtin_amdgcn_permlane16_swap(asu(s[0]), asu(s[1]), false, false);
  m = asf(r[0]) + asf(r[1]);
  r = __builtin_amdgcn_permlane32_swap(asu(m), asu(m), false, false);
  m = __builtin_amdgcn_rcpf(asf(r[0]) + asf(r[1]));
  r = __builtin_amdgcn_permlane16_swap(asu(m), asu(m), false, false);
  const f32x2 rr = {asf(r[0]), asf(r[1])};
#pragma unroll
  for (int i = 0; i < 4; ++i) { f32x2 t = e[i] * rr; mainv[i] = pk2(t[0], t[1]); }
  f32x2 t = e[4] * rr;
  extv = pk2(t[0], t[1]);
}
__device__ __forceinline__ unsigned silu2(float u0, float u1, float c1, float irs) {
  f32x2 u = {u0, u1}, c = {c1, c1}, ir = {irs, irs};
  f32x2 t = u * c;
  f32x2 e = {__builtin_amdgcn_exp2f(t[0]), __builtin_amdgcn_exp2f(t[1])};
  f32x2 d = __builtin_elementwise_fma(e, ir, ir);
  f32x2 rc = {__builtin_amdgcn_rcpf(d[0]), __builtin_amdgcn_rcpf(d[1])};
  f32x2 h = u * rc;
  return pk2(h[0], h[1]);
}

template <int TYPE>
__device__ __forceinline__ void k1_body(const char* __restrict__ x, const char* __restrict__ wqkv, const char* __restrict__ wphi,
                                        const char* __restrict__ wo, const char* __restrict__ w1, const char* __restrict__ w2,
                                        char* __restrict__ ws, int rb, int jj, int bid) {
  constexpr int NT = TYPE == 2 ? 8 : 7;
  const int tid = threadIdx.x, l = tid & 63;
  const int w = __builtin_amdgcn_readfirstlane(tid >> 6);
  const int tg = w & 3, nh = w >> 2;
  const int g = l >> 4, lc = l & 15, q = (l >> 2) & 3, p = l & 3;
  STAMP(0, 0);
  const __amdgpu_buffer_rsrc_t rs = RSRC(ws);
  const char* xp;
  {
    int row = 8 * w + (l >> 3);
    xp = x + (size_t)(rb * 128 + row) * 1280 + (((l & 7) ^ (l >> 3)) << 4);
  }
  auto wsrc = [&](int row, int sl, int& ld) -> const char* {
    const char* wp;
    if (TYPE == 0) {
      if (sl >= 24) { wp = w1 + jj * 128 + (sl - 24) * 16; ld = 3456; }
      else { int s2 = sl < 11 ? (sl > 9 ? 9 : sl) : (sl > 20 ? 9 : sl - 11);
             wp = wqkv + (sl < 11 ? 1280 : 2560) + jj * 160 + s2 * 16; ld = 3840; }
    } else if (TYPE == 1) {
      if (sl >= 24) { wp = w1 + (8 + jj) * 128 + (sl - 24) * 16; ld = 3456; }
      else { int s2 = sl > 19 ? 19 : sl; wp = wqkv + jj * 320 + s2 * 16; ld = 3840; }
    } else {
      int L = 12 + 4 * jj + (sl >> 3); L = L > 26 ? 26 : L;
      wp = w1 + L * 128 + (sl & 7) * 16; ld = 3456;
    }
    return wp + (size_t)row * ld;
  };
  int ld;
  const char* wp;
  {
    int row = 2 * w + (l >> 5);
    wp = wsrc(row, (l & 31) ^ (((row & 3) << 1) | (((row >> 3) & 1) << 3)), ld);
  }
  unsigned tch = 0;
  auto touch = [&]() {
    const int st = w < 7 ? 3 + w : 9;
    const int c8 = l & 7;
    const int sl = TYPE == 0 ? (c8 < 3 ? (c8 * 9 + 1) / 2 : (c8 < 6 ? 11 + ((c8 - 3) * 9 + 1) / 2 : 24 + (c8 - 6) * 4)) : (c8 * 4 + (c8 == 5 ? -1 : 0));
    int ld2;
    const char* a = wsrc(st * 64 + (rb & 7) * 8 + (l >> 3), sl, ld2);
    const char* b = x + (size_t)(rb * 128 + ((bid >> 3) & 3) * 32 + (l & 31)) * 1280 + st * 128;
    asm volatile("global_load_dword %0, %1, off\n\tglobal_load_dword %0, %2, off" : "+v"(tch) : "v"(a), "v"(b));
  };
  auto issue = [&](int s, int buf) {
    unsigned b = buf * STG;
    const char* xs = xp + s * 128;
    glds16(xs, b + w * 1024);
    glds16(xs + 64 * 1280, b + 8192 + w * 1024);
    const char* ws_ = wp + (size_t)(s * 64) * ld;
#pragma unroll
    for (int m = 0; m < 4; ++m) glds16(ws_ + (size_t)(16 * m) * ld, b + 16384 + (w + 8 * m) * 1024);
  };
  auto issue1 = [&](int s, int buf, int i) {
    unsigned b = buf * STG;
    if (i < 2) glds16(xp + s * 128 + i * 64 * 1280, b + i * 8192 + w * 1024);
    else glds16(wp + (size_t)(s * 64 + 16 * (i - 2)) * ld, b + 16384 + (w + 8 * (i - 2)) * 1024);
  };
  issue(0, 0); issue(1, 1); issue(2, 2);
  unsigned ta[NT], ca[2];
  {
    unsigned rowb = 16384 + (8 * g + q) * 512, sw = (q << 2) | ((g & 1) << 4);
    auto grp = [&](int t) -> int {
      if (TYPE == 0) {
        if (t < 2) return 5 * (2 * nh + t) + p;
        if (t == 2) return (p >> 1) * 22 + 10 * nh + 5 * (p & 1) + 4;
        if (t < 5) return 22 + 5 * (2 * nh + t - 3) + p;
        return 48 + 4 * (2 * nh + t - 5) + p;
      } else if (TYPE == 1) {
        if (t < 4) return 5 * (4 * nh + t) + p;
        if (t == 4) return 20 * nh + 5 * p + 4;
        return 48 + 4 * (2 * nh + t - 5) + p;
      } else return 32 * nh + 4 * t + p;
    };
#pragma unroll
    for (int t = 0; t < NT; ++t) ta[t] = rowb + ((grp(t) ^ sw) << 3);
#pragma unroll
    for (int c = 0; c < 2; ++c) {
      int t = 2 * tg + c; t = t >= NT ? NT - 1 : t;
      ca[c] = rowb + ((grp(t) ^ sw) << 3);
    }
  }
  unsigned xa[2][2];
#pragma unroll
  for (int tt = 0; tt < 2; ++tt)
#pragma unroll
    for (int k2 = 0; k2 < 2; ++k2) {
      int row = 32 * tg + 16 * (tt ^ nh) + lc;
      xa[tt][k2] = row * 128 + (((4 * k2 + g) ^ (row & 7)) << 4);
    }
  f32x4 acc[NT][2];
#pragma unroll
  for (int t = 0; t < NT; ++t) { acc[t][0] = (f32x4)(0.f); acc[t][1] = (f32x4)(0.f); }
  unsigned one2 = 0x3f803f80u;
  asm volatile("" : "+v"(one2));
  const int su0 = (rb * 4 + jj) * 6;
  auto side_issue = [&](int u, unsigned lb) {
    int ks = u / 5, cb = u - ks * 5;
    int v = 4 * w + (l >> 4);
    const char* src;
    if (ks < 54) src = w2 + (size_t)(32 * ks + v) * 1280;
    else {
      int k2 = ks - 54, hg = k2 / 5, kk = k2 - hg * 5, row;
      if (kk < 4) row = 20 * (8 * hg + 2 * kk + (v >> 4)) + (v & 15);
      else row = 20 * (8 * hg + 2 * ((v & 15) >> 2) + (v >> 4)) + 16 + (v & 3);
      src = wo + (size_t)row * 1280;
    }
    glds16(src + cb * 256 + (((l & 15) ^ ((v & 7) << 1)) << 4), lb + w * 1024);
  };

  constexpr int NF = NT + 2;
  unsigned fa[NF];
#pragma unroll
  for (int t = 0; t < NT; ++t) fa[t] = ta[t];
  fa[NT] = ca[0]; fa[NT + 1] = ca[1];
  u32x2 wl[2][NF], wh[2][NF];
  u32x4 xf[2][2];
#define RD(S, K2, I)                                                                                              \
  {                                                                                                               \
    if ((I) < 2) asm volatile("ds_read_b128 %0, %1" : "=v"(xf[S][(I) & 1]) : "v"(xa[(I) & 1][K2]));               \
    else if ((I) < 2 + 2 * NF) {                                                                                  \
      if (((I) & 1) == 0) asm volatile("ds_read_b64_tr_b16 %0, %1 offset:%2" : "=v"(wl[S][((I) - 2) >> 1 < NF ? ((I) - 2) >> 1 : 0]) : "v"(fa[((I) - 2) >> 1 < NF ? ((I) - 2) >> 1 : 0]), "i"((K2) * 16384)); \
      else asm volatile("ds_read_b64_tr_b16 %0, %1 offset:%2" : "=v"(wh[S][((I) - 2) >> 1 < NF ? ((I) - 2) >> 1 : 0]) : "v"(fa[((I) - 2) >> 1 < NF ? ((I) - 2) >> 1 : 0]), "i"((K2) * 16384 + 2048)); \
    }                                                                                                             \
  }
#define TIE(S)                                                                                                    \
  {                                                                                                               \
    asm volatile("" : "+v"(xf[S][0]), "+v"(xf[S][1]));                                                            \
    _Pragma("unroll") for (int t = 0; t < NF; ++t) asm volatile("" : "+v"(wl[S][t]), "+v"(wh[S][t]));             \
  }
#define NDBL 5
#define PRE1 0
  f32x4 mcs[2] = {(f32x4)(0.f), (f32x4)(0.f)}, mrs = (f32x4)(0.f), mrq = (f32x4)(0.f);
  const u32x4 ones = {one2, one2, one2, one2};
#define WFR(S, T) ((u32x4){wl[S][T][0], wl[S][T][1], wh[S][T][0], wh[S][T][1]})
#define CUM(I) ((I) + ((I) < NDBL ? (I) : NDBL) + ((I) > 3) + ((I) > 6) + ((I) > 9) + ((I) > 12))
#define PW(S, T) asm volatile("s_waitcnt lgkmcnt(12)" : "+v"(wl[S][T]), "+v"(wh[S][T]))
#define SLOT(S, K2N, DORD, I)                                                                                     \
  {                                                                                                               \
    if (DORD) { RD((S) ^ 1, K2N, CUM(I)); if ((I) < NDBL) RD((S) ^ 1, K2N, CUM(I) + 1); }                         \
    if ((I) == 3 || (I) == 6 || (I) == 9 || (I) == 12) {                                                          \
      __builtin_amdgcn_sched_barrier(0);                                                                          \
      if ((I) == 3) mrs = mfma32(ones, xf[S][0], mrs);                                                            \
      if ((I) == 6) mrq = mfma32(xf[S][0], xf[S][0], mrq);                                                        \
      if ((I) == 9) { if ((S) == 0) PW(S, NT); mcs[0] = mfma32(WFR(S, NT), ones, mcs[0]); }                       \
      if ((I) == 12) { if ((S) == 0) PW(S, NT + 1); mcs[1] = mfma32(WFR(S, NT + 1), ones, mcs[1]); }              \
      if (DORD) RD((S) ^ 1, K2N, CUM((I) + 1) - 1);                                                               \
    }                                                                                                             \
  }
#define KSTEP(S, K2N, DORD, GL)                                                                                   \
  {                                                                                                               \
    _Pragma("unroll") for (int t = 0; t < NT; ++t) {                                                              \
      if ((S) == 0) PW(S, t);                                                                                     \
      u32x4 wf = {wl[S][t][0], wl[S][t][1], wh[S][t][0], wh[S][t][1]};                                            \
      acc[t][0] = mfma32(wf, xf[S][0], acc[t][0]);                                                                \
      SLOT(S, K2N, DORD, 2 * t);                                                                                  \
      __builtin_amdgcn_sched_barrier(0);                                                                          \
      acc[t][1] = mfma32(wf, xf[S][1], acc[t][1]);                                                                \
      SLOT(S, K2N, DORD, 2 * t + 1);                                                                              \
      if ((GL) >= 0 && t >= 1 && t < 7) issue1(GL, (GL) % 3, t - 1);                                              \
      if ((GL) == -2 && TYPE == 1 && t >= 1 && t < 7) side_issue(su0 + t - 1 < 370 ? su0 + t - 1 : 369, STG + (t - 1) * 8192); \
      if ((GL) == -2 && TYPE == 0 && t == 1) { int c = tid < 200 ? tid : 199; if (w < 4) glds16(wphi + (size_t)jj * 3200 + c * 16, L_WPHI + w * 1024); } \
      __builtin_amdgcn_sched_barrier(0);                                                                          \
    }                                                                                                             \
  }
  if (PRE1 == 1) { touch(); WAITVM(14); } else WAITVM(12);
  BAR();
  STAMP(0, 1);
  if (PRE1 == 2) touch();
#pragma unroll
  for (int i = 0; i < 2 + 2 * NF; ++i) RD(0, 0, i);
#pragma unroll
  for (int s = 0; s < 10; ++s) {
    asm volatile("s_waitcnt lgkmcnt(12)" : "+v"(xf[0][0]), "+v"(xf[0][1]));
    __builtin_amdgcn_sched_barrier(0);
    KSTEP(0, 1, true, -1);
    WAITLGKM0();
    TIE(1);
    if (s < 9) {
      if (PRE1 && s < 2) WAITVM(8); else if (s < 8 || TYPE == 1) WAITVM(6); else if (TYPE == 0 && w < 4) WAITVM(1); else WAITVM(0);
      if (s == 2) asm volatile("" ::"v"(tch));
      BAR();
      const int d = (s % 3 == 2) ? -2 * STG : STG;
#pragma unroll
      for (int t = 0; t < NF; ++t) fa[t] += d;
      xa[0][0] += d; xa[0][1] += d; xa[1][0] += d; xa[1][1] += d;
    }
    __builtin_amdgcn_sched_barrier(0);
    KSTEP(1, 0, s < 9, (s <= 6 ? s + 3 : (s == 7 ? -2 : -1)));
  }
  STAMP(0, 2);
  {
    if (lc < 2) *(LDSP(f32x4))(L_CS + nh * 512 + (2 * tg + lc) * 64 + g * 16) = lc ? mcs[1] : mcs[0];
    float sm = mrs[0], sq = p == 0 ? mrq[0] : (p == 1 ? mrq[1] : (p == 2 ? mrq[2] : mrq[3]));
    float mean_ = sm * (1.0f / 640.0f);
    float var = fmaxf(sq * (1.0f / 640.0f) - mean_ * mean_, 0.f) + 1e-5f;
    f32x2 st = {mean_, __builtin_amdgcn_rsqf(var)};
    if (g == q) *(LDSP(f32x2))(L_ST + (32 * tg + 16 * nh + lc) * 8) = st;
  }
  WAITVM(0);
  WAITLGKM0();
  BAR();
  float mean[2], rstd[2];
#pragma unroll
  for (int tt = 0; tt < 2; ++tt) {
    f32x2 st = *(LDSP(f32x2))(L_ST + (32 * tg + 16 * (tt ^ nh) + lc) * 8);
    mean[tt] = st[0]; rstd[tt] = st[1];
  }
#pragma unroll
  for (int t = 0; t < NT; ++t) {
    f32x4 c4 = *(LDSP(f32x4))(L_CS + nh * 512 + t * 64 + g * 16);
#pragma unroll
    for (int tt = 0; tt < 2; ++tt) {
      f32x4 mm = {-mean[tt], -mean[tt], -mean[tt], -mean[tt]};
      acc[t][tt] = __builtin_elementwise_fma(mm, c4, acc[t][tt]);
    }
  }
  STAMP(0, 3);
  if (TYPE == 1) {
    unsigned a = STG + (4 * g + q) * 256 + (((2 * w + (p >> 1)) ^ (((4 * g + q) & 7) << 1)) << 4) + (p & 1) * 8;
#pragma unroll
    for (int i = 0; i < 6; ++i) {
      if (su0 + i < 370) {
        int u = su0 + i, ks = u / 5, cb = u - ks * 5, ct = cb * 8 + w, nb = ct / 5, ci = ct - nb * 5;
        u32x4 f = trd2(a + i * 8192, a + i * 8192 + 4096);
        ST16(WS_BF + ((nb * 74 + ks) * 5 + ci) * 1024 + l * 16, f);
      }
    }
  }
  {
    constexpr int T0 = TYPE == 2 ? 0 : 5;
#pragma unroll
    for (int t = T0; t < NT; t += 2) {
      int ks;
      if (TYPE == 0) ks = 2 * jj + nh;
      else if (TYPE == 1) ks = 2 * (8 + jj) + nh;
      else ks = 2 * (12 + 4 * jj) + 4 * nh + (t >> 1);
      if (ks < 54) {
#pragma unroll
        for (int tt = 0; tt < 2; ++tt) {
          const float c1 = rstd[tt] * -1.4426950408889634f, irs = __builtin_amdgcn_rcpf(rstd[tt]);
          u32x4 o = {silu2(acc[t][tt][0], acc[t][tt][1], c1, irs), silu2(acc[t][tt][2], acc[t][tt][3], c1, irs),
                     silu2(acc[t + 1][tt][0], acc[t + 1][tt][1], c1, irs), silu2(acc[t + 1][tt][2], acc[t + 1][tt][3], c1, irs)};
          int gtt = rb * 8 + 2 * tg + (tt ^ nh);
          ST16(WS_HF + (gtt * 54 + ks) * 1024 + l * 16, o);
        }
      }
    }
  }
  STAMP(0, 4);
  if (TYPE == 2) return;
  if (TYPE == 1) {
    const int tp = rb * 4 + tg;
#pragma unroll
    for (int t = 0; t < 5; ++t) {
      f32x4 v0 = acc[t][0] * rstd[0], v1 = acc[t][1] * rstd[1];
      u32x2 a = {pk2(v0[0], v0[1]), pk2(v0[2], v0[3])}, b = {pk2(v1[0], v1[1]), pk2(v1[2], v1[3])};
      u32x4 o = nh ? (u32x4){b[0], b[1], a[0], a[1]} : (u32x4){a[0], a[1], b[0], b[1]};
      int dst = t < 4 ? (int)WS_QF + ((jj * 8 + nh * 4 + t) * 64 + tp) * 1024 : (int)WS_QX + ((jj * 2 + nh) * 64 + tp) * 1024;
      ST16(dst + l * 16, o);
    }
    {
      const u32x4 sen = {0xffffffffu, 0xffffffffu, 0xffffffffu, 0xffffffffu};
      int dst = (int)WS_OF + (rb * 4 + jj) * 40960 + tid * 16;
#pragma unroll
      for (int i = 0; i < 5; ++i) ST16(dst + i * 8192, sen);
    }
    STAMP(0, 5);
    return;
  }
  u32x2 xb[2];
#pragma unroll
  for (int tt = 0; tt < 2; ++tt) { xb[tt][0] = pk2(acc[2][tt][0], acc[2][tt][1]); xb[tt][1] = pk2(acc[2][tt][2], acc[2][tt][3]); }
#pragma unroll
  for (int hh = 0; hh < 2; ++hh) {
    const int hl = 2 * nh + hh;
    unsigned wb = L_WPHI + hl * 800;
    u32x4 a0 = trd2(wb + (4 * g + q) * 40 + p * 8, wb + (16 + q) * 40 + p * 8);
    u32x4 a1;
    {
      unsigned b1 = wb + 4 * g * 40 + 32 + q * 2, b2 = wb + 640 + 32 + q * 2;
      a1[0] = (unsigned)*(LDSP(unsigned short))(b1) | ((unsigned)*(LDSP(unsigned short))(b1 + 40) << 16);
      a1[1] = (unsigned)*(LDSP(unsigned short))(b1 + 80) | ((unsigned)*(LDSP(unsigned short))(b1 + 120) << 16);
      a1[2] = (unsigned)*(LDSP(unsigned short))(b2) | ((unsigned)*(LDSP(unsigned short))(b2 + 40) << 16);
      a1[3] = (unsigned)*(LDSP(unsigned short))(b2 + 80) | ((unsigned)*(LDSP(unsigned short))(b2 + 120) << 16);
    }
    if (rb == 0 && tg == 0) {
      int wf = (int)WS_WF + (jj * 4 + hl) * 2048 + l * 16;
      ST16(wf, a0); ST16(wf + 1024, a1);
    }
    if (g != hh) { a0[2] = 0; a0[3] = 0; a1[2] = 0; a1[3] = 0; }
#pragma unroll
    for (int tt = 0; tt < 2; ++tt) {
      u32x4 kb = {pk2(acc[hh][tt][0], acc[hh][tt][1]), pk2(acc[hh][tt][2], acc[hh][tt][3]), xb[tt][0], xb[tt][1]};
      f32x4 pm = mfma32(a0, kb, (f32x4)(0.f));
      f32x4 px = mfma32(a1, kb, (f32x4)(0.f));
      u32x4 mv; unsigned ev;
      hedge(pm, px[0], rstd[tt] * 1.4426950408889634f, mv, ev);
      unsigned ib = (nh ? 0 : 2 * STG) + hh * KVI + (32 * tg + 16 * (tt ^ nh) + lc) * RS;
      *(LDSP(u32x4))(ib + g * 16) = mv;
      *(LDSP(unsigned))(ib + 64 + g * 4) = ev;
      f32x4 v = acc[3 + hh][tt] * rstd[tt];
      u32x2 vv = {pk2(v[0], v[1]), pk2(v[2], v[3])};
      *(LDSP(u32x2))(ib + 80 + g * 8) = vv;
    }
  }
  STAMP(0, 5);
  {
#pragma unroll
    for (int tt = 0; tt < 2; ++tt) {
      unsigned ib = (nh ? 0 : 2 * STG) + (g & 1) * KVI + (32 * tg + 16 * (tt ^ nh) + lc) * RS + (g >= 2 ? 112 : 120);
      f32x4 v = acc[2][tt] * rstd[tt];
      u32x2 vv = {pk2(v[0], v[1]), pk2(v[2], v[3])};
      if (g < 2) { vv[0] = 0x00003f80u; vv[1] = 0u; }
      *(LDSP(u32x2))(ib) = vv;
    }
    WAITVM(0);
    WAITLGKM0();
    BAR();
    if (tid < 2) flag_set(ws, 2048 + tid * 256 + rb * 16 + (jj >> 1) * 4 + (jj & 1));
    const int hl = w >> 1, dt = w & 1;
    unsigned ib = (hl < 2 ? 2 * STG : 0) + (hl & 1) * KVI + (4 * g + q) * RS + p * 8;
    f32x4 kv0 = (f32x4)(0.f), kv1 = (f32x4)(0.f), kv2 = (f32x4)(0.f);
#pragma unroll
    for (int k = 0; k < 4; ++k) {
      unsigned a = ib + k * 32 * RS;
      u32x4 bv = trd2(a + 80 + dt * 32, a + 80 + dt * 32 + 16 * RS);
      kv0 = mfma32(trd2(a, a + 16 * RS), bv, kv0);
      kv1 = mfma32(trd2(a + 32, a + 32 + 16 * RS), bv, kv1);
      kv2 = mfma32(trd2(a + 64, a + 64 + 16 * RS), bv, kv2);
    }
    if (dt == 0 || lc < 5) {
      int col = 16 * dt + lc;
      int o = (int)WS_KVP + ((jj * 4 + hl) * 16 + rb) * 3360 + col * 160 + g * 16;
      ST16(o, kv0);
      ST16(o + 64, kv1);
      if (g < 2) ST16(o + 128, kv2);
    }
    STAMP(0, 6);
  }
}

constexpr int L2_G = 102400;
constexpr int L2_S = 97280;
constexpr int K2_LDS = 163840;
#define PF 3
#define HP 2
#define SLP2 4
#define WOD 58
#define WB 90
#define PRE 0
#define PRE0 6
#define SLP3 2
#define GB 0
#define AUX 16
#define ROT 0
#define ROTX 0
#define MAP 1
#define SLP0 8
#define SLP1 4
__device__ __forceinline__ void lds_wait(LDSP(unsigned) f, unsigned v) {
  for (int sp = 0; sp < (1 << 22); ++sp) {
    if (__hip_atomic_load(f, __ATOMIC_RELAXED, __HIP_MEMORY_SCOPE_WORKGROUP) >= v) break;
    __builtin_amdgcn_s_sleep(1);
  }
  asm volatile("" ::: "memory");
}
__global__ __launch_bounds__(512, 2) void kf(const char* __restrict__ x, const char* __restrict__ wqkv, const char* __restrict__ wphi,
                                             const char* __restrict__ wo, const char* __restrict__ w1, const char* __restrict__ w2,
                                             char* __restrict__ ws, char* __restrict__ out) {
  unsigned long long pc;
  asm volatile("s_getpc_b64 %0" : "=s"(pc));
  const int bid = blockIdx.x, xcd = bid & 7, idx = bid >> 3;
  if (threadIdx.x < CODE_PF / 2048) {
    const unsigned long long a = (pc & ~255ull) + (unsigned)((idx * (CODE_PF / 2048) + threadIdx.x) * 64);
    asm volatile("s_mov_b32 m0, %1\n\ts_nop 0\n\tglobal_load_lds_dword %0, off" ::"v"(a), "s"(K1_LDS) : "memory");
  }
  {
    const int rb = (xcd >> 2) * 8 + (idx >> 2), j = xcd & 3, ty = idx & 3;
    if (ty < 2) k1_body<0>(x, wqkv, wphi, wo, w1, w2, ws, rb, 2 * j + ty, bid);
    else if (ty == 2) k1_body<1>(x, wqkv, wphi, wo, w1, w2, ws, rb, j, bid);
    else k1_body<2>(x, wqkv, wphi, wo, w1, w2, ws, rb, j, bid);
    __builtin_amdgcn_s_waitcnt(0);
    WAITVM(0);
    BAR();
    const int t = threadIdx.x;
    if (ty < 2) { if (t < 4) flag_set(ws, 2560 + t * 128 + (2 * j + ty) * 16 + rb); }
    else if (t < 2) flag_set(ws, 2048 + t * 256 + rb * 16 + j * 4 + ty);
    else if (ty == 2 && t < 34) flag_set(ws, (t - 2) * 64 + rb * 4 + j);
  }
  const __amdgpu_buffer_rsrc_t rs = RSRC(ws);
  const int mb = 8 * (xcd >> 1) + (idx >> 2), nb = 4 * (xcd & 1) + ((idx + (idx >> 2)) & 3);
  const int tgp = idx >> 2;
  const int tid = threadIdx.x, l = tid & 63;
  const int w = __builtin_amdgcn_readfirstlane(tid >> 6);
  const int g = l >> 4, lc = l & 15;
  const unsigned lo = l * 16;
  STAMP(1, 0);
  LDSP(unsigned) flg = (LDSP(unsigned))(L2_S + 2048);
  if (w < 4) {
    __builtin_amdgcn_s_setprio(HP);
    const int head = xcd * 4 + (idx & 3);
    if (tid < 8) flg[tid] = 0u;
    WAITLGKM0();
    BAR();
    lds_wait(flg + 1, 1u);
    u32x4 qf[2], qx[2];
#pragma unroll
    for (int i = 0; i < 2; ++i) {
      int tp = tgp * 8 + w * 2 + i;
      qf[i] = LD16(WS_QF + (head * 64 + tp) * 1024 + lo);
      qx[i] = LD16(WS_QX + ((head >> 2) * 64 + tp) * 1024 + lo);
    }
    if (w == 0) {
      const unsigned long long hi = tag_hi();
      for (int sp = 0; sp < (1 << 18); ++sp) {
        bool ok = flag_ok(ws, 2560 + (idx & 3) * 128 + xcd * 16 + lc, hi);
        if (__builtin_amdgcn_ballot_w64(!ok) == 0ull) break;
        __builtin_amdgcn_s_sleep(4);
      }
      if (l == 0) __hip_atomic_store(flg + 4, 1u, __ATOMIC_RELAXED, __HIP_MEMORY_SCOPE_WORKGROUP);
    } else lds_wait(flg + 4, 1u);
    asm volatile("" ::: "memory");
    STAMP(1, 7);
    u32x4 a0 = LD16(WS_WF + head * 2048 + lo), a1 = LD16(WS_WF + head * 2048 + 1024 + lo);
    f32x4 part[16];
    const int kp = (int)WS_KVP + head * 16 * 3360 + (tid < 210 ? tid : 209) * 16;
#pragma unroll
    for (int i = 0; i < 16; ++i) part[i] = __builtin_bit_cast(f32x4, LD16(kp + i * 3360));
    __builtin_amdgcn_sched_barrier(0);
    if (g != (head & 3)) { a0[2] = 0; a0[3] = 0; a1[2] = 0; a1[3] = 0; }
    u32x4 pq[4]; unsigned px[4];
#pragma unroll
    for (int i = 0; i < 4; ++i) {
      u32x4 kb = {qf[i >> 1][2 * (i & 1)], qf[i >> 1][2 * (i & 1) + 1], qx[i >> 1][2 * (i & 1)], qx[i >> 1][2 * (i & 1) + 1]};
      f32x4 pm = mfma32(a0, kb, (f32x4)(0.f));
      f32x4 pe = mfma32(a1, kb, (f32x4)(0.f));
      hedge(pm, pe[0], 1.4426950408889634f, pq[i], px[i]);
    }
    __builtin_amdgcn_sched_barrier(0);
    f32x4 s = ((part[0] + part[1]) + (part[2] + part[3])) + ((part[4] + part[5]) + (part[6] + part[7])) +
              (((part[8] + part[9]) + (part[10] + part[11])) + ((part[12] + part[13]) + (part[14] + part[15])));
    u32x2 sb = {pk2(s[0], s[1]), pk2(s[2], s[3])};
    if (tid < 210) *(LDSP(u32x2))(L2_S + tid * 8) = sb;
    WAITLGKM0();
    if (l == 0) __hip_atomic_fetch_add(flg, 1u, __ATOMIC_RELAXED, __HIP_MEMORY_SCOPE_WORKGROUP);
    lds_wait(flg, 4u);
    STAMP(1, 1);
    {
      const int c1 = 16 + (lc & 3);
      u32x4 b0 = lds128(L2_S + lc * 80 + g * 16), b1 = lds128(L2_S + c1 * 80 + g * 16), b2 = lds128(L2_S + 1600 + g * 16);
      u32x4 e0 = {*(LDSP(unsigned))(L2_S + lc * 80 + 64 + g * 4), 0u, 0u, 0u}, e1 = {*(LDSP(unsigned))(L2_S + c1 * 80 + 64 + g * 4), 0u, 0u, 0u},
            e2 = {*(LDSP(unsigned))(L2_S + 1600 + 64 + g * 4), 0u, 0u, 0u};
      unsigned long long oe = 0;
#pragma unroll
      for (int i = 0; i < 4; ++i) {
        u32x4 pe = {px[i], 0u, 0u, 0u};
        f32x4 d2 = mfma32(b2, pq[i], (f32x4)(0.f));
        f32x4 d0 = mfma32(b0, pq[i], (f32x4)(0.f));
        f32x4 d1 = mfma32(b1, pq[i], (f32x4)(0.f));
        d2 = mfma32(e2, pe, d2);
        d0 = mfma32(e0, pe, d0);
        d1 = mfma32(e1, pe, d1);
        float inv = __builtin_amdgcn_rcpf(d2[0] + 1e-6f);
        unsigned long long ov = (unsigned long long)pk2(d0[0] * inv, d0[1] * inv) | ((unsigned long long)pk2(d0[2] * inv, d0[3] * inv) << 32);
        unsigned long long ev = (unsigned long long)pk2(d1[0] * inv, d1[1] * inv) | ((unsigned long long)pk2(d1[2] * inv, d1[3] * inv) << 32);
        if (g == i) oe = ev;
        int gtt = tgp * 16 + w * 4 + i;
        __hip_atomic_store((unsigned long long*)(ws + WS_OF + ((size_t)head * 128 + gtt) * 512 + l * 8), ov, __ATOMIC_RELAXED, __HIP_MEMORY_SCOPE_AGENT);
      }
      __hip_atomic_store((unsigned long long*)(ws + WS_OX + ((size_t)((tgp * 16 + w * 4 + g) * 4 + (head >> 3)) * 2 + (head & 1)) * 512 + ((head & 7) >> 1) * 128 + lc * 8),
                         oe, __ATOMIC_RELAXED, __HIP_MEMORY_SCOPE_AGENT);
    }
    STAMP(1, 2);
    __builtin_amdgcn_sched_barrier(0);
    u32x4 bl[5];
#pragma unroll
    for (int c = 0; c < 5; ++c) bl[c] = LD16(WS_BF + (nb * 74 + 73) * 5120 + c * 1024 + lo);
    const size_t orow = (size_t)(mb * 64 + w * 16 + lc) * 1280 + nb * 160 + g * 8;
    u32x2 xv[5];
#pragma unroll
    for (int c = 0; c < 5; ++c) xv[c] = *(const u32x2*)(x + orow + c * 32);
    __builtin_amdgcn_sched_barrier(0);
    unsigned long long om[32], ox[8];
    {
      const char* op = ws + WS_OF + (size_t)(mb * 4 + w) * 512 + l * 8;
      const char* oxp = ws + WS_OX + (size_t)(mb * 4 + w) * 4096 + l * 8;
      unsigned need = 15u;
      __builtin_amdgcn_s_sleep(SLP0);
      for (int sp = 0; sp < (1 << 16); ++sp) {
#pragma unroll
        for (int hg = 0; hg < 4; ++hg)
          if (need & (1u << hg)) {
#pragma unroll
            for (int j = 0; j < 8; ++j)
              om[8 * hg + j] = __hip_atomic_load((const unsigned long long*)(op + (size_t)(8 * hg + j) * 65536), __ATOMIC_RELAXED, __HIP_MEMORY_SCOPE_AGENT);
#pragma unroll
            for (int j = 0; j < 2; ++j)
              ox[2 * hg + j] = __hip_atomic_load((const unsigned long long*)(oxp + (2 * hg + j) * 512), __ATOMIC_RELAXED, __HIP_MEMORY_SCOPE_AGENT);
          }
#pragma unroll
        for (int hg = 0; hg < 4; ++hg)
          if (need & (1u << hg)) {
            unsigned m = max(max((unsigned)ox[2 * hg], (unsigned)(ox[2 * hg] >> 32)), max((unsigned)ox[2 * hg + 1], (unsigned)(ox[2 * hg + 1] >> 32)));
#pragma unroll
            for (int j = 0; j < 8; ++j) m = max(max(m, (unsigned)om[8 * hg + j]), (unsigned)(om[8 * hg + j] >> 32));
            if (__builtin_amdgcn_ballot_w64(m == 0xffffffffu) == 0ull) need &= ~(1u << hg);
          }
        if (need == 0u) break;
        __builtin_amdgcn_s_sleep(SLP1);
      }
    }
    STAMP(1, 3);
    __builtin_amdgcn_sched_barrier(0);
    lds_wait(flg + 3, 4u);
    u32x4 wf[3][5];
#pragma unroll
    for (int k = 0; k < 3; ++k)
#pragma unroll
      for (int c = 0; c < 5; ++c) wf[k][c] = lds128((k * 5 + c) * 1024 + lo);
    STAMP(1, 6);
    f32x4 acc[5];
#pragma unroll
    for (int c = 0; c < 5; ++c) acc[c] = (f32x4)(0.f);
#pragma unroll
    for (int k = 0; k < 20; ++k) {
      const int hg = k / 5, kk = k % 5;
      const unsigned long long f0 = kk < 4 ? om[8 * hg + 2 * (kk & 3)] : ox[2 * hg], f1 = kk < 4 ? om[8 * hg + 2 * (kk & 3) + 1] : ox[2 * hg + 1];
      u32x4 af = {(unsigned)f0, (unsigned)(f0 >> 32), (unsigned)f1, (unsigned)(f1 >> 32)};
#pragma unroll
      for (int c = 0; c < 5; ++c) {
        acc[c] = mfma32(k < 19 ? wf[k % 3][c] : bl[c], af, acc[c]);
        if (k + 3 < 19) wf[k % 3][c] = lds128(((k + 3) * 5 + c) * 1024 + lo);
        __builtin_amdgcn_sched_barrier(0);
      }
    }
    __builtin_amdgcn_s_setprio(0);
    if (l == 0) __hip_atomic_fetch_add(flg + 2, 1u, __ATOMIC_RELAXED, __HIP_MEMORY_SCOPE_WORKGROUP);
    STAMP(1, 4);
    BAR();
#pragma unroll
    for (int c = 0; c < 5; ++c) {
      unsigned a = L2_G + (w * 5 + c) * 1024 + lo;
      f32x4 gv = (*(LDSP(f32x4))(a) + *(LDSP(f32x4))(a + 20480)) + (*(LDSP(f32x4))(a + 40960) + *(LDSP(f32x4))(a - L2_G));
      float o[4];
#pragma unroll
      for (int r = 0; r < 4; ++r) {
        float xf = __builtin_bit_cast(float, (r & 1) ? (xv[c][r >> 1] & 0xffff0000u) : (xv[c][r >> 1] << 16));
        o[r] = __builtin_fmaf(acc[c][r], silu_f(gv[r]), xf);
      }
      u32x2 ov = {pk2(o[0], o[1]), pk2(o[2], o[3])};
      *(u32x2*)(out + orow + c * 32) = ov;
    }
    STAMP(1, 5);
  } else {
    f32x4 acc[4][5];
#pragma unroll
    for (int i = 0; i < 4; ++i)
#pragma unroll
      for (int c = 0; c < 5; ++c) acc[i][c] = (f32x4)(0.f);
    const int k0 = (w - 4) * 14 - (w == 7 ? 1 : 0);
    const int nk = w < 6 ? 14 : 13;
    const int rot = ROT ? (2 * (idx & 7) + 7 * ((idx >> 3) & 1) + 3 * (idx >> 4) + ROTX * xcd) % 13 : 0;
    auto kidx = [&](int k) { int kn = (k < nk ? k : nk - 1) + rot; return kn >= nk ? kn - nk : kn; };
    const int ap = (int)WS_HF + (mb * 4 * 54 + k0) * 1024, bp = (int)WS_BF + (nb * 74 + k0) * 5120;
#define BLD(OFF) __builtin_bit_cast(u32x4, __builtin_amdgcn_raw_buffer_load_b128(rs, lo, (OFF), AUX))
    u32x4 af[PF][4], bfr[PF][5];
    const int f0 = w == 7 ? 71 : (w - 4) * 24;
    const char* wp = ws + WS_BF + (size_t)(nb * 74 + 54) * 5120 + f0 * 1024 + lo;
    BAR();
    if (w == 4) {
      const unsigned long long hi = tag_hi();
      for (int sp = 0; sp < (1 << 18); ++sp) {
        bool ok = flag_ok(ws, idx * 64 + l, hi) & flag_ok(ws, 2048 + (mb & 1) * 256 + (mb >> 1) * 16 + lc, hi);
        if (__builtin_amdgcn_ballot_w64(!ok) == 0ull) break;
        __builtin_amdgcn_s_sleep(4);
      }
      if (l == 0) __hip_atomic_store(flg + 1, 1u, __ATOMIC_RELAXED, __HIP_MEMORY_SCOPE_WORKGROUP);
    } else lds_wait(flg + 1, 1u);
    asm volatile("" ::: "memory");
    STAMP(1, 7);
#pragma unroll
    for (int i0 = 0; i0 < 6; ++i0) {
      const int i = ROT ? (i0 + (idx & 7)) % 6 : i0;
      const char* a = wp + i * 4096;
      asm volatile("s_mov_b32 m0, %1\n\ts_nop 0\n\tglobal_load_lds_dwordx4 %0, off sc1\n\tglobal_load_lds_dwordx4 %0, off offset:1024 sc1\n\t"
                   "global_load_lds_dwordx4 %0, off offset:2048 sc1\n\tglobal_load_lds_dwordx4 %0, off offset:3072 sc1"
                   ::"v"(a), "s"((f0 + 4 * i) * 1024) : "memory");
    }
#pragma unroll
    for (int k = 0; k < PF; ++k) {
      const int kn = kidx(k);
#pragma unroll
      for (int c = 0; c < 5; ++c) bfr[k][c] = BLD(bp + kn * 5120 + c * 1024);
#pragma unroll
      for (int i = 0; i < 4; ++i) af[k][i] = BLD(ap + i * 55296 + kn * 1024);
    }
    __builtin_amdgcn_sched_barrier(0);
#pragma unroll
    for (int k = 0; k < 14; ++k) {
      if (k == 13 && w >= 6) {
#pragma unroll
        for (int i = 0; i < 4; ++i) af[k % PF][i] = (u32x4)(0u);
      }
#pragma unroll
      for (int i = 0; i < 4; ++i)
#pragma unroll
        for (int c = 0; c < 5; ++c) acc[i][c] = mfma32(bfr[k % PF][c], af[k % PF][i], acc[i][c]);
      __builtin_amdgcn_sched_barrier(0);
      if (k == 0 && l == 0) __hip_atomic_fetch_add(flg + 3, 1u, __ATOMIC_RELAXED, __HIP_MEMORY_SCOPE_WORKGROUP);
      if (k + PF < 14) {
        const int kn = kidx(k + PF);
#pragma unroll
        for (int c = 0; c < 5; ++c) bfr[k % PF][c] = BLD(bp + kn * 5120 + c * 1024);
#pragma unroll
        for (int i = 0; i < 4; ++i) af[k % PF][i] = BLD(ap + i * 55296 + kn * 1024);
      }
      __builtin_amdgcn_sched_barrier(0);
    }
    STAMP(1, 4);
    if (w == 7) lds_wait(flg + 2, 4u);
    unsigned base = (w == 7 ? 0 : L2_G + (w - 4) * 20480) + lo;
#pragma unroll
    for (int i = 0; i < 4; ++i)
#pragma unroll
      for (int c = 0; c < 5; ++c) *(LDSP(f32x4))(base + (i * 5 + c) * 1024) = acc[i][c];
    WAITLGKM0();
    BAR();
  }
}

extern "C" void kernel_launch(void* const* d_in, const int* in_sizes, int n_in, void* d_out, int out_size, void* d_ws, size_t ws_size,
                              hipStream_t stream) {
  (void)in_sizes; (void)n_in; (void)out_size; (void)ws_size;
  hipFuncSetAttribute((const void*)kf, hipFuncAttributeMaxDynamicSharedMemorySize, K2_LDS);
  kf<<<256, 512, K2_LDS, stream>>>((const char*)d_in[0], (const char*)d_in[1], (const char*)d_in[2], (const char*)d_in[3],
                                   (const char*)d_in[5], (const char*)d_in[6], (char*)d_ws, (char*)d_out);
}
```
